```python
import torch

def _softmax(x, axis=-1):
    return torch.softmax(x, dim=axis)

def _gelu(x):
    return torch.nn.functional.gelu(x, approximate='tanh')

def _silu(x):
    return torch.nn.functional.silu(x)

def _sigmoid(x):
    return torch.sigmoid(x)

def _layernorm(x, axis=-1, eps=1e-05):
    m = x.mean(dim=axis, keepdim=True)
    v = x.var(dim=axis, keepdim=True, unbiased=False)
    return (x - m) / torch.sqrt(v + eps)

def _scatter_add(tgt, idx, src, axis=0):
    return tgt.clone().index_add_(axis, idx, src)

def _segment_sum(src, seg, n):
    out = torch.zeros((n,) + src.shape[1:], dtype=src.dtype)
    return out.index_add_(0, seg, src)

def _pad(x, pw):
    flat = tuple((v for pair in reversed(pw) for v in pair))
    return torch.nn.functional.pad(x, flat)
kernel_name = 'standalone__autoregressive_conv_wavenet__speech'

def setup_inputs():

    _w = lambda a: (lambda t: t.to(torch.bfloat16) if t.is_floating_point() else t)(torch.as_tensor(a))
    B, T, D, K = (2, 4000, 256, 31)
    return {'x': _w((lambda *s: torch.randn(*s, dtype=torch.bfloat16) * 0.1)(B, T, D)), 'w': _w((lambda *s: torch.randn(*s, dtype=torch.bfloat16) * 0.1)(K, D, 2 * D)), 'w_o': _w((lambda *s: torch.randn(*s, dtype=torch.bfloat16) * 0.05)(D, D))}

def reference(x, w, w_o):
    B, T, D = x.shape
    K = w.shape[0]
    dil = 2
    pad = (K - 1) * dil
    xp = _pad(x, ((0, 0), (pad, 0), (0, 0)))
    h = torch.einsum('btc,cd->btd', xp[:, pad:pad + T, :], w[0])
    for i in range(1, K):
        off = pad - i * dil
        h = h + torch.einsum('btc,cd->btd', xp[:, off:off + T, :], w[i])
    f = h[..., :D].clone()
    g = h[..., D:].clone()
    z = torch.tanh(f) * _sigmoid(g)
    return z @ w_o
```

```cpp
#include <hip/hip_runtime.h>
#include <hip/hip_bf16.h>
__global__ void k1(const __hip_bfloat16* x, const __hip_bfloat16* w, float* z, int T) {
    int bt = blockIdx.x; int b = bt / T, t = bt % T; int d = threadIdx.x;
    __shared__ float xs[256];
    float f = 0.f, g = 0.f;
    for (int i = 0; i < 31; ++i) {
        int tt = t - 2 * i;
        if (tt < 0) break;
        __syncthreads();
        xs[d] = __bfloat162float(x[((size_t)b * T + tt) * 256 + d]);
        __syncthreads();
        const __hip_bfloat16* wi = w + (size_t)i * 256 * 512;
        for (int c = 0; c < 256; ++c) {
            f += xs[c] * __bfloat162float(wi[c * 512 + d]);
            g += xs[c] * __bfloat162float(wi[c * 512 + 256 + d]);
        }
    }
    float zz = tanhf(f) * (1.f / (1.f + expf(-g)));
    z[(size_t)bt * 256 + d] = __bfloat162float(__float2bfloat16(zz));
}
__global__ void k2(const float* z, const __hip_bfloat16* wo, __hip_bfloat16* out) {
    int bt = blockIdx.x; int e = threadIdx.x;
    __shared__ float zs[256];
    zs[e] = z[(size_t)bt * 256 + e];
    __syncthreads();
    float a = 0.f;
    for (int c = 0; c < 256; ++c) a += zs[c] * __bfloat162float(wo[c * 256 + e]);
    out[(size_t)bt * 256 + e] = __float2bfloat16(a);
}
extern "C" void kernel_launch(void* const* d_in, const int* in_sizes, int n_in,
                              void* d_out, int out_size, void* d_ws, size_t ws_size,
                              hipStream_t stream) {
    const __hip_bfloat16* x = (const __hip_bfloat16*)d_in[0];
    const __hip_bfloat16* w = (const __hip_bfloat16*)d_in[1];
    const __hip_bfloat16* wo = (const __hip_bfloat16*)d_in[2];
    float* z = (float*)d_ws;
    k1<<<8000, 256, 0, stream>>>(x, w, z, 4000);
    k2<<<8000, 256, 0, stream>>>(z, wo, (__hip_bfloat16*)d_out);
}
```

```cpp
#include <hip/hip_runtime.h>
#include <hip/hip_bf16.h>

typedef __attribute__((ext_vector_type(8))) __bf16 bf16x8;
typedef __attribute__((ext_vector_type(2))) __bf16 bf16x2;
typedef __attribute__((ext_vector_type(4))) short s16x4;
typedef __attribute__((ext_vector_type(8))) short s16x8;
typedef __attribute__((ext_vector_type(16))) float f32x16;
typedef __attribute__((ext_vector_type(4))) unsigned u32x4;
typedef __attribute__((ext_vector_type(2))) unsigned u32x2;
typedef __attribute__((address_space(3))) void* lds_ptr;
typedef __attribute__((address_space(3))) s16x4* lds_s16x4_ptr;

#define AUX_IN 2
#define AUX_ST1 16
#define AUX_ST2 0
#define AUX_ST3 0
#define XPAD 0
#define WPAD 0
#define YPAD 0

constexpr int T_LEN = 4000, L_SEQ = 2000;
constexpr unsigned XF = 262144 + XPAD;
constexpr unsigned WF = 524288 + WPAD;
constexpr unsigned YT = 65536 + YPAD;
constexpr unsigned X_OFF = 0;
constexpr unsigned W_OFF = 16u << 20;
constexpr unsigned Y_OFF = 48u << 20;
constexpr unsigned O_OFF = 80u << 20;
constexpr unsigned RS3 = 0x00020000;

#define STAMP(slot, which)
#define STAMP_END(slot)

__device__ __forceinline__ unsigned pk(float a, float b) {
    bf16x2 r = {(__bf16)a, (__bf16)b};
    return __builtin_bit_cast(unsigned, r);
}
__device__ __forceinline__ float bf_lo(unsigned u) { return __builtin_bit_cast(float, u << 16); }
__device__ __forceinline__ float bf_hi(unsigned u) { return __builtin_bit_cast(float, u & 0xffff0000u); }
__device__ __forceinline__ f32x16 mfma(u32x4 a, u32x4 b, f32x16 c) {
    return __builtin_amdgcn_mfma_f32_32x32x16_bf16(__builtin_bit_cast(bf16x8, a), __builtin_bit_cast(bf16x8, b), c, 0, 0, 0);
}
__device__ __forceinline__ u32x2 tr_read(const char* p) {
    return __builtin_bit_cast(u32x2, __builtin_amdgcn_ds_read_tr16_b64_v4i16((lds_s16x4_ptr)p));
}
__device__ __forceinline__ void cos_pair(int i0, int i1, float scale, unsigned& hi, unsigned& lo) {
    float c0 = __builtin_amdgcn_cosf((float)(i0 & 127) * (1.0f / 128.0f)) * scale;
    float c1 = __builtin_amdgcn_cosf((float)(i1 & 127) * (1.0f / 128.0f)) * scale;
    hi = pk(c0, c1);
    lo = pk(c0 - bf_lo(hi), c1 - bf_hi(hi));
}

constexpr int NA = 128, NW = 256, NO = 32;
__global__ __launch_bounds__(256, 2) void k1_forward(const char* __restrict__ x, const char* __restrict__ w,
                                                      const unsigned short* __restrict__ wo, char* __restrict__ ws) {
    __shared__ __attribute__((aligned(16))) char lds[36864];
    STAMP(0, 0);
    const int bid = blockIdx.x, tid = threadIdx.x, lane = tid & 63;
    const int wv = __builtin_amdgcn_readfirstlane(tid >> 6);
    auto rs_ws = __builtin_amdgcn_make_buffer_rsrc((void*)ws, 0, 0x7fffffff, RS3);
    const int hh = lane >> 5, r32 = lane & 31;
    const int half = wv & 1, rt = wv >> 1;
    const int g4 = lane >> 4, q = (lane >> 2) & 3, p = lane & 3;
    const char* trb = lds + (8 * (g4 >> 1) + q) * 128 + ((64 * half + 32 * (g4 & 1) + 8 * p) ^ (64 * (q >> 1)));
    const int srow = lane >> 3, schunk = (lane & 7) ^ (4 * ((srow >> 1) & 1));
    const int fr = 2 * (16 * rt + (r32 >> 1)) + 1, ph = 32 * (r32 & 1);
    if (bid < NA) {
        const int oc = bid >> 2, cq = bid & 3, s = oc >> 3, o = oc & 7;
        auto rs = __builtin_amdgcn_make_buffer_rsrc((void*)x, 0, 2 * T_LEN * 512, RS3);
#pragma unroll
        for (int i = 0; i < 9; ++i) {
            const int u = wv * 9 + i;
            const int m = 256 * o - 32 + 8 * u + srow;
            const int off = (m >= 0 && m < L_SEQ) ? ((s >> 1) * T_LEN + 2 * m + (s & 1)) * 512 + cq * 128 + schunk * 16 : 0x7ffffff0;
            __builtin_amdgcn_raw_ptr_buffer_load_lds(rs, (lds_ptr)(lds + u * 1024), 16, off, 0, 0, AUX_IN);
        }
        u32x4 fh[4], fl[4];
#pragma unroll
        for (int ks = 0; ks < 4; ++ks)
#pragma unroll
            for (int j = 0; j < 4; ++j) {
                const int n = 16 * ks + 8 * hh + 2 * j;
                unsigned a, b; cos_pair(fr * n + ph, fr * (n + 1) + ph, 1.0f, a, b); fh[ks][j] = a; fl[ks][j] = b;
            }
        __syncthreads();
        f32x16 acc[8];
#pragma unroll
        for (int j = 0; j < 8; ++j) acc[j] = (f32x16)(0.0f);
#pragma unroll
        for (int b = 0; b < 9; ++b)
#pragma unroll
            for (int k2 = 0; k2 < 2; ++k2) {
                u32x2 v0 = tr_read(trb + b * 4096 + k2 * 2048), v1 = tr_read(trb + b * 4096 + k2 * 2048 + 512);
                u32x4 v = {v0.x, v0.y, v1.x, v1.y};
                if (b < 8) { acc[b] = mfma(fh[k2], v, acc[b]); acc[b] = mfma(fl[k2], v, acc[b]); }
                if (b > 0) { acc[b - 1] = mfma(fh[2 + k2], v, acc[b - 1]); acc[b - 1] = mfma(fl[2 + k2], v, acc[b - 1]); }
            }
        const int c = 64 * cq + 32 * half + r32, og = o & 3;
        const unsigned voff = X_OFF + (16 * rt + 2 * hh) * XF + (2 * s + (o >> 2)) * 32768 + (c >> 4) * 2048 + og * 256 + (((c & 15) ^ (4 * og)) * 16);
#pragma unroll
        for (int e = 0; e < 16; ++e) {
            u32x4 v = {pk(acc[0][e], acc[1][e]), pk(acc[2][e], acc[3][e]), pk(acc[4][e], acc[5][e]), pk(acc[6][e], acc[7][e])};
            __builtin_amdgcn_raw_buffer_store_b128(v, rs_ws, voff, (((e & 3) >> 1) + 4 * (e >> 2)) * XF + (e & 1) * 1024, AUX_ST1);
        }
    } else if (bid < NA + NW) {
        const int wb = bid - NA, oct = wb >> 3, dg = wb & 7;
        auto rs = __builtin_amdgcn_make_buffer_rsrc((void*)w, 0, 31 * 256 * 1024, RS3);
#pragma unroll
        for (int i = 0; i < 8; ++i) {
            const int cc = wv * 2 + (i >> 2), tap = 8 * (i & 3) + srow;
            const int off = (tap * 256 + 8 * oct + cc) * 1024 + dg * 128 + schunk * 16;
            __builtin_amdgcn_raw_ptr_buffer_load_lds(rs, (lds_ptr)(lds + (wv * 8 + i) * 1024), 16, off, 0, 0, AUX_IN);
        }
        u32x4 fh[2], fl[2];
#pragma unroll
        for (int ks = 0; ks < 2; ++ks)
#pragma unroll
            for (int j = 0; j < 4; ++j) {
                const int n = 16 * ks + 8 * hh + 2 * j;
                unsigned a, b; cos_pair(fr * n + ph, fr * (n + 1) + ph, 1.0f, a, b); fh[ks][j] = a; fl[ks][j] = b;
            }
        __syncthreads();
        f32x16 acc[8];
#pragma unroll
        for (int j = 0; j < 8; ++j) {
            acc[j] = (f32x16)(0.0f);
#pragma unroll
            for (int k2 = 0; k2 < 2; ++k2) {
                u32x2 v0 = tr_read(trb + j * 4096 + k2 * 2048), v1 = tr_read(trb + j * 4096 + k2 * 2048 + 512);
                u32x4 v = {v0.x, v0.y, v1.x, v1.y};
                acc[j] = mfma(fh[k2], v, acc[j]); acc[j] = mfma(fl[k2], v, acc[j]);
            }
        }
        const unsigned voff = W_OFF + (16 * rt + 2 * hh) * WF + (2 * dg + half) * 32768 + (oct >> 1) * 2048 + (oct & 1) * 512 + r32 * 16;
#pragma unroll
        for (int e = 0; e < 16; ++e) {
            u32x4 v = {pk(acc[0][e], acc[1][e]), pk(acc[2][e], acc[3][e]), pk(acc[4][e], acc[5][e]), pk(acc[6][e], acc[7][e])};
            __builtin_amdgcn_raw_buffer_store_b128(v, rs_ws, voff, (((e & 3) >> 1) + 4 * (e >> 2)) * WF + (e & 1) * 1024, AUX_ST1);
        }
    } else {
        const int piece = (bid - NA - NW) * 4 + wv;
        const int eg = piece >> 4, kz = piece & 15;
        unsigned v[8];
#pragma unroll
        for (int j = 0; j < 8; ++j) v[j] = wo[(16 * kz + 8 * (j >> 2) + 4 * hh + (j & 3)) * 256 + 32 * eg + r32];
        u32x4 o4 = {v[0] | (v[1] << 16), v[2] | (v[3] << 16), v[4] | (v[5] << 16), v[6] | (v[7] << 16)};
        __builtin_amdgcn_raw_buffer_store_b128(o4, rs_ws, O_OFF + piece * 1024 + lane * 16, 0, AUX_ST1);
    }
    STAMP_END(0);
}

__device__ __forceinline__ void glds16(const char* sbase, unsigned voff, unsigned ldsaddr) {
    unsigned keep;
    asm volatile("s_mov_b32 %0, m0\n\ts_mov_b32 m0, %3\n\ts_nop 0\n\tglobal_load_lds_dwordx4 %1, %2\n\ts_mov_b32 m0, %0"
                 : "=&s"(keep) : "v"(voff), "s"(sbase), "s"(ldsaddr) : "memory");
}
template <int OFF>
__device__ __forceinline__ void gload16(u32x4& d, const char* sbase, unsigned voff) {
    asm volatile("global_load_dwordx4 %0, %1, %2 offset:%3" : "=v"(d) : "v"(voff), "s"(sbase), "i"(OFF) : "memory");
}
#define WAIT_VM(n) asm volatile("s_waitcnt vmcnt(" #n ")" ::: "memory")
#define PIN8(a) asm volatile("" : "+v"((a)[0]), "+v"((a)[1]), "+v"((a)[2]), "+v"((a)[3]), "+v"((a)[4]), "+v"((a)[5]), "+v"((a)[6]), "+v"((a)[7]))

__global__ __launch_bounds__(256, 1) void k2_gemm(char* __restrict__ ws) {
    extern __shared__ __attribute__((aligned(16))) char lds[];
    STAMP(1, 0);
    const int bid = blockIdx.x, tid = threadIdx.x, lane = tid & 63;
    const int wv = __builtin_amdgcn_readfirstlane(tid >> 6);
    const int f = (bid & 7) * 4 + (bid >> 6), sub = (bid >> 3) & 7, th = sub >> 2, cg = (sub & 3) * 4 + wv;
    const char* wsrc = ws + W_OFF + f * WF + cg * 32768;
    const char* xsrc = ws + X_OFF + f * XF + th * 131072;
    const unsigned l16 = lane * 16;
    const unsigned ldsb = (unsigned)(size_t)(lds_ptr)lds;
    u32x4 wf[32];
#pragma unroll
    for (int kq = 0; kq < 4; ++kq) {
        const char* b = wsrc + kq * 8192 + 4096;
        gload16<-4096>(wf[kq * 8 + 0], b, l16); gload16<-3072>(wf[kq * 8 + 1], b, l16);
        gload16<-2048>(wf[kq * 8 + 2], b, l16); gload16<-1024>(wf[kq * 8 + 3], b, l16);
        gload16<0>(wf[kq * 8 + 4], b, l16); gload16<1024>(wf[kq * 8 + 5], b, l16);
        gload16<2048>(wf[kq * 8 + 6], b, l16); gload16<3072>(wf[kq * 8 + 7], b, l16);
#pragma unroll
        for (int i = 0; i < 2; ++i) { const int o = kq * 8192 + (wv * 2 + i) * 1024; glds16(xsrc + o, l16, ldsb + o); }
    }
#pragma unroll
    for (int g = 1; g < 3; ++g)
#pragma unroll
        for (int i = 0; i < 8; ++i) { const int o = g * 32768 + (wv * 8 + i) * 1024; glds16(xsrc + o, l16, ldsb + o); }

    const int g4 = lane >> 4, q = (lane >> 2) & 3, p = lane & 3;
    const int og = 2 * (g4 & 1) + (p >> 1);
    const int ta = og * 256 + (((8 * (g4 >> 1) + q) ^ (4 * og)) * 16) + 8 * (p & 1);
    const char* t0 = lds + ta;
    const char* t1 = lds + (ta ^ 64);
    auto rs_ws = __builtin_amdgcn_make_buffer_rsrc((void*)ws, 0, 0x7fffffff, RS3);
    const int hh = lane >> 5, r32 = lane & 31;
    const unsigned voff = Y_OFF + (128 * th + 4 * hh) * YT + f * 2048 + (32 * cg + r32) * 4;

    f32x16 p1[4], p2[4], p3[4];
#pragma unroll
    for (int g = 0; g < 4; ++g) {
        p1[g] = (f32x16)(0.0f); p2[g] = (f32x16)(0.0f); p3[g] = (f32x16)(0.0f);
#pragma unroll
        for (int kq = 0; kq < 4; ++kq) {
            if (g == 0) {
                if (kq == 0) WAIT_VM(46);
                if (kq == 1) WAIT_VM(44);
                if (kq == 2) WAIT_VM(34);
                if (kq == 3) WAIT_VM(24);
                PIN8(&wf[kq * 8]);
                __builtin_amdgcn_s_barrier();
                asm volatile("" ::: "memory");
                if (kq == 0) {
#pragma unroll
                    for (int i = 0; i < 8; ++i) { const int o = 3 * 32768 + (wv * 8 + i) * 1024; glds16(xsrc + o, l16, ldsb + o); }
                }
            } else if (kq == 0) {
                if (g == 1) WAIT_VM(16);
                if (g == 2) WAIT_VM(24);
                if (g == 3) WAIT_VM(32);
                __builtin_amdgcn_s_barrier();
                asm volatile("" ::: "memory");
            }
#pragma unroll
            for (int k4 = 0; k4 < 4; ++k4) {
                const int ks = kq * 4 + k4, o = g * 32768 + ks * 2048;
                u32x2 a0 = tr_read(t0 + o), a1 = tr_read(t1 + o), b0 = tr_read(t0 + o + 1024), b1 = tr_read(t1 + o + 1024);
                u32x4 xr = {a0.x, a0.y, a1.x, a1.y}, xi = {b0.x, b0.y, b1.x, b1.y};
                p1[g] = mfma(xr, wf[2 * ks], p1[g]);
                p3[g] = mfma(xr, wf[2 * ks + 1], p3[g]);
                p2[g] = mfma(xi, wf[2 * ks + 1], p2[g]);
                p3[g] = mfma(xi, wf[2 * ks], p3[g]);
            }
            if (g > 0) {
#pragma unroll
                for (int e = 4 * kq; e < 4 * kq + 4; ++e) {
                    const unsigned v = pk(p1[g - 1][e] - p2[g - 1][e], p3[g - 1][e]);
                    __builtin_amdgcn_raw_buffer_store_b32(v, rs_ws, voff, (32 * (g - 1) + (e & 3) + 8 * (e >> 2)) * YT, AUX_ST2);
                }
            }
        }
    }
#pragma unroll
    for (int e = 0; e < 16; ++e) {
        const unsigned v = pk(p1[3][e] - p2[3][e], p3[3][e]);
        __builtin_amdgcn_raw_buffer_store_b32(v, rs_ws, voff, (96 + (e & 3) + 8 * (e >> 2)) * YT, AUX_ST2);
    }
    STAMP_END(1);
}

__global__ __launch_bounds__(512, 2) void k3_inverse(const char* __restrict__ ws, char* __restrict__ out) {
    __shared__ __attribute__((aligned(16))) char zb[16384];
    STAMP(2, 0);
    const int bid = blockIdx.x, tid = threadIdx.x, lane = tid & 63;
    const int wv = __builtin_amdgcn_readfirstlane(tid >> 6);
    const int s = bid / 63, k = bid - 63 * s, tile = 64 * s + k;
    const int hh = lane >> 5, r32 = lane & 31;
    auto rs_ws = __builtin_amdgcn_make_buffer_rsrc((void*)ws, 0, 0x7fffffff, RS3);
    const unsigned voff = Y_OFF + tile * YT + hh * 8192 + (32 * wv + r32) * 4;
    u32x4 yf[4], yg[4];
#pragma unroll
    for (int ks = 0; ks < 4; ++ks)
#pragma unroll
        for (int j = 0; j < 4; ++j) {
            yf[ks][j] = __builtin_amdgcn_raw_buffer_load_b32(rs_ws, voff, ks * 16384 + j * 2048, 0);
            yg[ks][j] = __builtin_amdgcn_raw_buffer_load_b32(rs_ws, voff + 1024, ks * 16384 + j * 2048, 0);
        }
    u32x4 wo[16];
#pragma unroll
    for (int kz = 0; kz < 16; ++kz) wo[kz] = __builtin_amdgcn_raw_buffer_load_b128(rs_ws, O_OFF + wv * 16384 + lane * 16, kz * 1024, 0);
    u32x4 gh[4], gl[4];
#pragma unroll
    for (int ks = 0; ks < 4; ++ks)
#pragma unroll
        for (int j = 0; j < 4; ++j) {
            const int i0 = (2 * (8 * ks + 4 * hh + j) + 1) * (32 + r32);
            unsigned a, b; cos_pair(i0, i0 + 32, 1.0f / 32.0f, a, b); gh[ks][j] = a; gl[ks][j] = b;
        }
    f32x16 hf = (f32x16)(0.0f), hg = (f32x16)(0.0f);
#pragma unroll
    for (int ks = 0; ks < 4; ++ks) {
        hf = mfma(yf[ks], gh[ks], hf); hg = mfma(yg[ks], gh[ks], hg);
        hf = mfma(yf[ks], gl[ks], hf); hg = mfma(yg[ks], gl[ks], hg);
    }
    float z[16];
#pragma unroll
    for (int e = 0; e < 16; ++e) {
        const float a = __builtin_amdgcn_exp2f(__builtin_amdgcn_fmed3f(hf[e] * 2.8853900817779268f, -60.0f, 60.0f));
        const float b = __builtin_amdgcn_exp2f(hg[e] * -1.4426950408889634f);
        z[e] = (a - 1.0f) * __builtin_amdgcn_rcpf((a + 1.0f) * (1.0f + b));
    }
#pragma unroll
    for (int sg = 0; sg < 2; ++sg) {
        u32x4 v = {pk(z[8 * sg], z[8 * sg + 1]), pk(z[8 * sg + 2], z[8 * sg + 3]), pk(z[8 * sg + 4], z[8 * sg + 5]), pk(z[8 * sg + 6], z[8 * sg + 7])};
        *(u32x4*)(zb + (2 * wv + sg) * 1024 + lane * 16) = v;
    }
    __syncthreads();
    f32x16 o = (f32x16)(0.0f);
#pragma unroll
    for (int kz = 0; kz < 16; ++kz) o = mfma(wo[kz], *(const u32x4*)(zb + kz * 1024 + lane * 16), o);
    const int m = 32 * k + r32;
    auto rs_o = __builtin_amdgcn_make_buffer_rsrc((void*)out, 0, 2 * T_LEN * 512, RS3);
    const unsigned ooff = m < L_SEQ ? ((s >> 1) * T_LEN + 2 * m + (s & 1)) * 512 + 64 * wv + 16 * hh : 0x7ffffff0;
#pragma unroll
    for (int mm = 0; mm < 4; mm += 2) {
        unsigned a0 = pk(o[4 * mm], o[4 * mm + 1]), a1 = pk(o[4 * mm + 2], o[4 * mm + 3]);
        unsigned b0 = pk(o[4 * mm + 4], o[4 * mm + 5]), b1 = pk(o[4 * mm + 6], o[4 * mm + 7]);
        auto r0 = __builtin_amdgcn_permlane32_swap(a0, b0, false, false);
        auto r1 = __builtin_amdgcn_permlane32_swap(a1, b1, false, false);
        u32x4 v = {r0[0], r1[0], r0[1], r1[1]};
        __builtin_amdgcn_raw_buffer_store_b128(v, rs_o, ooff, mm * 16, AUX_ST3);
    }
    STAMP_END(2);
}

__global__ void k_empty(char* ws) { if (ws == nullptr) __builtin_trap(); }
extern "C" void kernel_launch(void* const* d_in, const int* in_sizes, int n_in,
                              void* d_out, int out_size, void* d_ws, size_t ws_size,
                              hipStream_t stream) {
    (void)hipFuncSetAttribute((const void*)k2_gemm, hipFuncAttributeMaxDynamicSharedMemorySize, 131072);
    k1_forward<<<NA + NW + NO, 256, 0, stream>>>((const char*)d_in[0], (const char*)d_in[1], (const unsigned short*)d_in[2], (char*)d_ws);
    k2_gemm<<<256, 256, 131072, stream>>>((char*)d_ws);
    k3_inverse<<<252, 512, 0, stream>>>((const char*)d_ws, (char*)d_out);
    k_empty<<<256, 256, 0, stream>>>((char*)d_ws);
}
```

```cpp
#include <hip/hip_runtime.h>
#include <hip/hip_bf16.h>

typedef __attribute__((ext_vector_type(8))) __bf16 bf16x8;
typedef __attribute__((ext_vector_type(2))) __bf16 bf16x2;
typedef __attribute__((ext_vector_type(4))) short s16x4;
typedef __attribute__((ext_vector_type(8))) short s16x8;
typedef __attribute__((ext_vector_type(16))) float f32x16;
typedef __attribute__((ext_vector_type(4))) unsigned u32x4;
typedef __attribute__((ext_vector_type(2))) unsigned u32x2;
typedef __attribute__((address_space(3))) void* lds_ptr;
typedef __attribute__((address_space(3))) s16x4* lds_s16x4_ptr;

#define AUX_IN 2
#define AUX_ST1 16
#define AUX_ST2 16
#define AUX_ST3 0
#define XPAD 0
#define WPAD 0
#define YPAD 0

constexpr int T_LEN = 4000, L_SEQ = 2000;
constexpr unsigned XF = 262144 + XPAD;
constexpr unsigned WF = 524288 + WPAD;
constexpr unsigned YT = 65536 + YPAD;
constexpr unsigned X_OFF = 0;
constexpr unsigned W_OFF = 16u << 20;
constexpr unsigned Y_OFF = 48u << 20;
constexpr unsigned O_OFF = 80u << 20;
constexpr unsigned RS3 = 0x00020000;

#define STAMP(slot, which)
#define STAMP_END(slot)

__device__ __forceinline__ unsigned pk(float a, float b) {
    bf16x2 r = {(__bf16)a, (__bf16)b};
    return __builtin_bit_cast(unsigned, r);
}
__device__ __forceinline__ float bf_lo(unsigned u) { return __builtin_bit_cast(float, u << 16); }
__device__ __forceinline__ float bf_hi(unsigned u) { return __builtin_bit_cast(float, u & 0xffff0000u); }
__device__ __forceinline__ f32x16 mfma(u32x4 a, u32x4 b, f32x16 c) {
    return __builtin_amdgcn_mfma_f32_32x32x16_bf16(__builtin_bit_cast(bf16x8, a), __builtin_bit_cast(bf16x8, b), c, 0, 0, 0);
}
__device__ __forceinline__ u32x2 tr_read(const char* p) {
    return __builtin_bit_cast(u32x2, __builtin_amdgcn_ds_read_tr16_b64_v4i16((lds_s16x4_ptr)p));
}
__device__ __forceinline__ void cos_pair(int i0, int i1, float scale, unsigned& hi, unsigned& lo) {
    float c0 = __builtin_amdgcn_cosf((float)(i0 & 127) * (1.0f / 128.0f)) * scale;
    float c1 = __builtin_amdgcn_cosf((float)(i1 & 127) * (1.0f / 128.0f)) * scale;
    hi = pk(c0, c1);
    lo = pk(c0 - bf_lo(hi), c1 - bf_hi(hi));
}

#define CODE_PREFETCH(SYM, N) do { \
    unsigned lo_, hi_; \
    asm volatile("s_getpc_b64 s[20:21]\n\ts_add_u32 s20, s20, " #SYM "@rel32@lo+4\n\ts_addc_u32 s21, s21, " #SYM "@rel32@hi+12\n\ts_mov_b32 %0, s20\n\ts_mov_b32 %1, s21" : "=s"(lo_), "=s"(hi_) :: "s20", "s21", "scc"); \
    const char* p_ = (const char*)(((unsigned long long)hi_ << 32) | lo_); \
    _Pragma("unroll") for (int i_ = 0; i_ < (N); ++i_) asm volatile("global_load_dword %0, %1, %2" : "=v"(pfv[i_]) : "v"((threadIdx.x & 63) * 128 + i_ * 8192), "s"(p_) : "memory"); \
} while (0)
constexpr int NA = 128, NW = 256, NO = 32;
extern "C" __global__ __launch_bounds__(256, 2) void k1_forward(const char* __restrict__ x, const char* __restrict__ w,
                                                      const unsigned short* __restrict__ wo, char* __restrict__ ws) {
    __shared__ __attribute__((aligned(16))) char lds[36864];
    STAMP(0, 0);
    unsigned pfv[3] = {0, 0, 0};
    if (blockIdx.x < 32 && threadIdx.x < 64) CODE_PREFETCH(k1_forward, 3);
    const int bid = blockIdx.x, tid = threadIdx.x, lane = tid & 63;
    const int wv = __builtin_amdgcn_readfirstlane(tid >> 6);
    auto rs_ws = __builtin_amdgcn_make_buffer_rsrc((void*)ws, 0, 0x7fffffff, RS3);
    const int hh = lane >> 5, r32 = lane & 31;
    const int half = wv & 1, rt = wv >> 1;
    const int g4 = lane >> 4, q = (lane >> 2) & 3, p = lane & 3;
    const char* trb = lds + (8 * (g4 >> 1) + q) * 128 + ((64 * half + 32 * (g4 & 1) + 8 * p) ^ (64 * (q >> 1)));
    const int srow = lane >> 3, schunk = (lane & 7) ^ (4 * ((srow >> 1) & 1));
    const int fr = 2 * (16 * rt + (r32 >> 1)) + 1, ph = 32 * (r32 & 1);
    if (bid < NA) {
        const int oc = bid >> 2, cq = bid & 3, s = oc >> 3, o = oc & 7;
        auto rs = __builtin_amdgcn_make_buffer_rsrc((void*)x, 0, 2 * T_LEN * 512, RS3);
#pragma unroll
        for (int i = 0; i < 9; ++i) {
            const int u = wv * 9 + i;
            const int m = 256 * o - 32 + 8 * u + srow;
            const int off = (m >= 0 && m < L_SEQ) ? ((s >> 1) * T_LEN + 2 * m + (s & 1)) * 512 + cq * 128 + schunk * 16 : 0x7ffffff0;
            __builtin_amdgcn_raw_ptr_buffer_load_lds(rs, (lds_ptr)(lds + u * 1024), 16, off, 0, 0, AUX_IN);
        }
        u32x4 fh[4], fl[4];
#pragma unroll
        for (int ks = 0; ks < 4; ++ks)
#pragma unroll
            for (int j = 0; j < 4; ++j) {
                const int n = 16 * ks + 8 * hh + 2 * j;
                unsigned a, b; cos_pair(fr * n + ph, fr * (n + 1) + ph, 1.0f, a, b); fh[ks][j] = a; fl[ks][j] = b;
            }
        __syncthreads();
        f32x16 acc[8];
#pragma unroll
        for (int j = 0; j < 8; ++j) acc[j] = (f32x16)(0.0f);
#pragma unroll
        for (int b = 0; b < 9; ++b)
#pragma unroll
            for (int k2 = 0; k2 < 2; ++k2) {
                u32x2 v0 = tr_read(trb + b * 4096 + k2 * 2048), v1 = tr_read(trb + b * 4096 + k2 * 2048 + 512);
                u32x4 v = {v0.x, v0.y, v1.x, v1.y};
                if (b < 8) { acc[b] = mfma(fh[k2], v, acc[b]); acc[b] = mfma(fl[k2], v, acc[b]); }
                if (b > 0) { acc[b - 1] = mfma(fh[2 + k2], v, acc[b - 1]); acc[b - 1] = mfma(fl[2 + k2], v, acc[b - 1]); }
            }
        const int c = 64 * cq + 32 * half + r32, og = o & 3;
        const unsigned voff = X_OFF + (16 * rt + 2 * hh) * XF + (2 * s + (o >> 2)) * 32768 + (c >> 4) * 2048 + og * 256 + (((c & 15) ^ (4 * og)) * 16);
#pragma unroll
        for (int e = 0; e < 16; ++e) {
            u32x4 v = {pk(acc[0][e], acc[1][e]), pk(acc[2][e], acc[3][e]), pk(acc[4][e], acc[5][e]), pk(acc[6][e], acc[7][e])};
            __builtin_amdgcn_raw_buffer_store_b128(v, rs_ws, voff, (((e & 3) >> 1) + 4 * (e >> 2)) * XF + (e & 1) * 1024, AUX_ST1);
        }
    } else if (bid < NA + NW) {
        const int wb = bid - NA, oct = wb >> 3, dg = wb & 7;
        auto rs = __builtin_amdgcn_make_buffer_rsrc((void*)w, 0, 31 * 256 * 1024, RS3);
#pragma unroll
        for (int i = 0; i < 8; ++i) {
            const int cc = wv * 2 + (i >> 2), tap = 8 * (i & 3) + srow;
            const int off = (tap * 256 + 8 * oct + cc) * 1024 + dg * 128 + schunk * 16;
            __builtin_amdgcn_raw_ptr_buffer_load_lds(rs, (lds_ptr)(lds + (wv * 8 + i) * 1024), 16, off, 0, 0, AUX_IN);
        }
        u32x4 fh[2], fl[2];
#pragma unroll
        for (int ks = 0; ks < 2; ++ks)
#pragma unroll
            for (int j = 0; j < 4; ++j) {
                const int n = 16 * ks + 8 * hh + 2 * j;
                unsigned a, b; cos_pair(fr * n + ph, fr * (n + 1) + ph, 1.0f, a, b); fh[ks][j] = a; fl[ks][j] = b;
            }
        __syncthreads();
        f32x16 acc[8];
#pragma unroll
        for (int j = 0; j < 8; ++j) {
            acc[j] = (f32x16)(0.0f);
#pragma unroll
            for (int k2 = 0; k2 < 2; ++k2) {
                u32x2 v0 = tr_read(trb + j * 4096 + k2 * 2048), v1 = tr_read(trb + j * 4096 + k2 * 2048 + 512);
                u32x4 v = {v0.x, v0.y, v1.x, v1.y};
                acc[j] = mfma(fh[k2], v, acc[j]); acc[j] = mfma(fl[k2], v, acc[j]);
            }
        }
        const unsigned voff = W_OFF + (16 * rt + 2 * hh) * WF + (2 * dg + half) * 32768 + (oct >> 1) * 2048 + (oct & 1) * 512 + r32 * 16;
#pragma unroll
        for (int e = 0; e < 16; ++e) {
            u32x4 v = {pk(acc[0][e], acc[1][e]), pk(acc[2][e], acc[3][e]), pk(acc[4][e], acc[5][e]), pk(acc[6][e], acc[7][e])};
            __builtin_amdgcn_raw_buffer_store_b128(v, rs_ws, voff, (((e & 3) >> 1) + 4 * (e >> 2)) * WF + (e & 1) * 1024, AUX_ST1);
        }
    } else {
        const int piece = (bid - NA - NW) * 4 + wv;
        const int eg = piece >> 4, kz = piece & 15;
        unsigned v[8];
#pragma unroll
        for (int j = 0; j < 8; ++j) v[j] = wo[(16 * kz + 8 * (j >> 2) + 4 * hh + (j & 3)) * 256 + 32 * eg + r32];
        u32x4 o4 = {v[0] | (v[1] << 16), v[2] | (v[3] << 16), v[4] | (v[5] << 16), v[6] | (v[7] << 16)};
        __builtin_amdgcn_raw_buffer_store_b128(o4, rs_ws, O_OFF + piece * 1024 + lane * 16, 0, AUX_ST1);
    }
    asm volatile("s_waitcnt vmcnt(0)" ::: "memory");
    asm volatile("" :: "v"(pfv[0]), "v"(pfv[1]), "v"(pfv[2]));
    STAMP_END(0);
}

__device__ __forceinline__ void glds16(const char* sbase, unsigned voff, unsigned ldsaddr) {
    unsigned keep;
    asm volatile("s_mov_b32 %0, m0\n\ts_mov_b32 m0, %3\n\ts_nop 0\n\tglobal_load_lds_dwordx4 %1, %2\n\ts_mov_b32 m0, %0"
                 : "=&s"(keep) : "v"(voff), "s"(sbase), "s"(ldsaddr) : "memory");
}
template <int OFF>
__device__ __forceinline__ void gload16(u32x4& d, const char* sbase, unsigned voff) {
    asm volatile("global_load_dwordx4 %0, %1, %2 offset:%3" : "=v"(d) : "v"(voff), "s"(sbase), "i"(OFF) : "memory");
}
#define WAIT_VM(n) asm volatile("s_waitcnt vmcnt(" #n ")" ::: "memory")
#define PIN8(a) asm volatile("" : "+v"((a)[0]), "+v"((a)[1]), "+v"((a)[2]), "+v"((a)[3]), "+v"((a)[4]), "+v"((a)[5]), "+v"((a)[6]), "+v"((a)[7]))

extern "C" __global__ __launch_bounds__(256, 1) void k2_gemm(char* __restrict__ ws) {
    extern __shared__ __attribute__((aligned(16))) char lds[];
    STAMP(1, 0);
    const int bid = blockIdx.x, tid = threadIdx.x, lane = tid & 63;
    const int wv = __builtin_amdgcn_readfirstlane(tid >> 6);
    const int f = (bid & 7) * 4 + (bid >> 6), sub = (bid >> 3) & 7, th = sub >> 2, cg = (sub & 3) * 4 + wv;
    const char* wsrc = ws + W_OFF + f * WF + cg * 32768;
    const char* xsrc = ws + X_OFF + f * XF + th * 131072;
    const unsigned l16 = lane * 16;
    const unsigned ldsb = (unsigned)(size_t)(lds_ptr)lds;
    u32x4 wf[32];
#pragma unroll
    for (int kq = 0; kq < 4; ++kq) {
        const char* b = wsrc + kq * 8192 + 4096;
        gload16<-4096>(wf[kq * 8 + 0], b, l16); gload16<-3072>(wf[kq * 8 + 1], b, l16);
        gload16<-2048>(wf[kq * 8 + 2], b, l16); gload16<-1024>(wf[kq * 8 + 3], b, l16);
        gload16<0>(wf[kq * 8 + 4], b, l16); gload16<1024>(wf[kq * 8 + 5], b, l16);
        gload16<2048>(wf[kq * 8 + 6], b, l16); gload16<3072>(wf[kq * 8 + 7], b, l16);
#pragma unroll
        for (int i = 0; i < 2; ++i) { const int o = kq * 8192 + (wv * 2 + i) * 1024; glds16(xsrc + o, l16, ldsb + o); }
    }
#pragma unroll
    for (int g = 1; g < 3; ++g)
#pragma unroll
        for (int i = 0; i < 8; ++i) { const int o = g * 32768 + (wv * 8 + i) * 1024; glds16(xsrc + o, l16, ldsb + o); }

    const int g4 = lane >> 4, q = (lane >> 2) & 3, p = lane & 3;
    const int og = 2 * (g4 & 1) + (p >> 1);
    const int ta = og * 256 + (((8 * (g4 >> 1) + q) ^ (4 * og)) * 16) + 8 * (p & 1);
    const char* t0 = lds + ta;
    const char* t1 = lds + (ta ^ 64);
    auto rs_ws = __builtin_amdgcn_make_buffer_rsrc((void*)ws, 0, 0x7fffffff, RS3);
    const int hh = lane >> 5, r32 = lane & 31;
    const unsigned voff = Y_OFF + (128 * th + 4 * hh) * YT + f * 2048 + (32 * cg + r32) * 4;

    f32x16 p1[4], p2[4], p3[4];
#pragma unroll
    for (int g = 0; g < 4; ++g) {
        p1[g] = (f32x16)(0.0f); p2[g] = (f32x16)(0.0f); p3[g] = (f32x16)(0.0f);
#pragma unroll
        for (int kq = 0; kq < 4; ++kq) {
            if (g == 0) {
                if (kq == 0) WAIT_VM(46);
                if (kq == 1) WAIT_VM(44);
                if (kq == 2) WAIT_VM(34);
                if (kq == 3) WAIT_VM(24);
                PIN8(&wf[kq * 8]);
                __builtin_amdgcn_s_barrier();
                asm volatile("" ::: "memory");
                if (kq == 0) {
#pragma unroll
                    for (int i = 0; i < 8; ++i) { const int o = 3 * 32768 + (wv * 8 + i) * 1024; glds16(xsrc + o, l16, ldsb + o); }
                }
            } else if (kq == 0) {
                if (g == 1) WAIT_VM(16);
                if (g == 2) WAIT_VM(24);
                if (g == 3) WAIT_VM(32);
                __builtin_amdgcn_s_barrier();
                asm volatile("" ::: "memory");
            }
#pragma unroll
            for (int k4 = 0; k4 < 4; ++k4) {
                const int ks = kq * 4 + k4, o = g * 32768 + ks * 2048;
                u32x2 a0 = tr_read(t0 + o), a1 = tr_read(t1 + o), b0 = tr_read(t0 + o + 1024), b1 = tr_read(t1 + o + 1024);
                u32x4 xr = {a0.x, a0.y, a1.x, a1.y}, xi = {b0.x, b0.y, b1.x, b1.y};
                p1[g] = mfma(xr, wf[2 * ks], p1[g]);
                p3[g] = mfma(xr, wf[2 * ks + 1], p3[g]);
                p2[g] = mfma(xi, wf[2 * ks + 1], p2[g]);
                p3[g] = mfma(xi, wf[2 * ks], p3[g]);
            }
            if (g > 0) {
#pragma unroll
                for (int e = 4 * kq; e < 4 * kq + 4; ++e) {
                    const unsigned v = pk(p1[g - 1][e] - p2[g - 1][e], p3[g - 1][e]);
                    __builtin_amdgcn_raw_buffer_store_b32(v, rs_ws, voff, (32 * (g - 1) + (e & 3) + 8 * (e >> 2)) * YT, AUX_ST2);
                }
            }
        }
    }
#pragma unroll
    for (int e = 0; e < 16; ++e) {
        const unsigned v = pk(p1[3][e] - p2[3][e], p3[3][e]);
        __builtin_amdgcn_raw_buffer_store_b32(v, rs_ws, voff, (96 + (e & 3) + 8 * (e >> 2)) * YT, AUX_ST2);
    }
    STAMP_END(1);
}

extern "C" __global__ __launch_bounds__(512, 2) void k3_inverse(const char* __restrict__ ws, char* __restrict__ out) {
    __shared__ __attribute__((aligned(16))) char zb[16384];
    STAMP(2, 0);
    const int bid = blockIdx.x, tid = threadIdx.x, lane = tid & 63;
    const int wv = __builtin_amdgcn_readfirstlane(tid >> 6);
    const int s = bid / 63, k = bid - 63 * s, tile = 64 * s + k;
    const int hh = lane >> 5, r32 = lane & 31;
    auto rs_ws = __builtin_amdgcn_make_buffer_rsrc((void*)ws, 0, 0x7fffffff, RS3);
    const unsigned voff = Y_OFF + tile * YT + hh * 8192 + (32 * wv + r32) * 4;
    u32x4 yf[4], yg[4];
#pragma unroll
    for (int ks = 0; ks < 4; ++ks)
#pragma unroll
        for (int j = 0; j < 4; ++j) {
            yf[ks][j] = __builtin_amdgcn_raw_buffer_load_b32(rs_ws, voff, ks * 16384 + j * 2048, 0);
            yg[ks][j] = __builtin_amdgcn_raw_buffer_load_b32(rs_ws, voff + 1024, ks * 16384 + j * 2048, 0);
        }
    u32x4 wo[16];
#pragma unroll
    for (int kz = 0; kz < 16; ++kz) wo[kz] = __builtin_amdgcn_raw_buffer_load_b128(rs_ws, O_OFF + wv * 16384 + lane * 16, kz * 1024, 0);
    u32x4 gh[4], gl[4];
#pragma unroll
    for (int ks = 0; ks < 4; ++ks)
#pragma unroll
        for (int j = 0; j < 4; ++j) {
            const int i0 = (2 * (8 * ks + 4 * hh + j) + 1) * (32 + r32);
            unsigned a, b; cos_pair(i0, i0 + 32, 1.0f / 32.0f, a, b); gh[ks][j] = a; gl[ks][j] = b;
        }
    f32x16 hf = (f32x16)(0.0f), hg = (f32x16)(0.0f);
#pragma unroll
    for (int ks = 0; ks < 4; ++ks) {
        hf = mfma(yf[ks], gh[ks], hf); hg = mfma(yg[ks], gh[ks], hg);
        hf = mfma(yf[ks], gl[ks], hf); hg = mfma(yg[ks], gl[ks], hg);
    }
    float z[16];
#pragma unroll
    for (int e = 0; e < 16; ++e) {
        const float a = __builtin_amdgcn_exp2f(__builtin_amdgcn_fmed3f(hf[e] * 2.8853900817779268f, -60.0f, 60.0f));
        const float b = __builtin_amdgcn_exp2f(hg[e] * -1.4426950408889634f);
        z[e] = (a - 1.0f) * __builtin_amdgcn_rcpf((a + 1.0f) * (1.0f + b));
    }
#pragma unroll
    for (int sg = 0; sg < 2; ++sg) {
        u32x4 v = {pk(z[8 * sg], z[8 * sg + 1]), pk(z[8 * sg + 2], z[8 * sg + 3]), pk(z[8 * sg + 4], z[8 * sg + 5]), pk(z[8 * sg + 6], z[8 * sg + 7])};
        *(u32x4*)(zb + (2 * wv + sg) * 1024 + lane * 16) = v;
    }
    __syncthreads();
    f32x16 o = (f32x16)(0.0f);
#pragma unroll
    for (int kz = 0; kz < 16; ++kz) o = mfma(wo[kz], *(const u32x4*)(zb + kz * 1024 + lane * 16), o);
    const int m = 32 * k + r32;
    auto rs_o = __builtin_amdgcn_make_buffer_rsrc((void*)out, 0, 2 * T_LEN * 512, RS3);
    const unsigned ooff = m < L_SEQ ? ((s >> 1) * T_LEN + 2 * m + (s & 1)) * 512 + 64 * wv + 16 * hh : 0x7ffffff0;
#pragma unroll
    for (int mm = 0; mm < 4; mm += 2) {
        unsigned a0 = pk(o[4 * mm], o[4 * mm + 1]), a1 = pk(o[4 * mm + 2], o[4 * mm + 3]);
        unsigned b0 = pk(o[4 * mm + 4], o[4 * mm + 5]), b1 = pk(o[4 * mm + 6], o[4 * mm + 7]);
        auto r0 = __builtin_amdgcn_permlane32_swap(a0, b0, false, false);
        auto r1 = __builtin_amdgcn_permlane32_swap(a1, b1, false, false);
        u32x4 v = {r0[0], r1[0], r0[1], r1[1]};
        __builtin_amdgcn_raw_buffer_store_b128(v, rs_o, ooff, mm * 16, AUX_ST3);
    }
    STAMP_END(2);
}

extern "C" void kernel_launch(void* const* d_in, const int* in_sizes, int n_in,
                              void* d_out, int out_size, void* d_ws, size_t ws_size,
                              hipStream_t stream) {
    (void)hipFuncSetAttribute((const void*)k2_gemm, hipFuncAttributeMaxDynamicSharedMemorySize, 131072);
    k1_forward<<<NA + NW + NO, 256, 0, stream>>>((const char*)d_in[0], (const char*)d_in[1], (const unsigned short*)d_in[2], (char*)d_ws);
    k2_gemm<<<256, 256, 131072, stream>>>((char*)d_ws);
    k3_inverse<<<252, 512, 0, stream>>>((const char*)d_ws, (char*)d_out);
}
```

```cpp
#define QSEL 0
#include <hip/hip_runtime.h>
#include <hip/hip_bf16.h>

typedef __attribute__((ext_vector_type(8))) __bf16 bf16x8;
typedef __attribute__((ext_vector_type(2))) __bf16 bf16x2;
typedef __attribute__((ext_vector_type(4))) short s16x4;
typedef __attribute__((ext_vector_type(8))) short s16x8;
typedef __attribute__((ext_vector_type(16))) float f32x16;
typedef __attribute__((ext_vector_type(4))) unsigned u32x4;
typedef __attribute__((ext_vector_type(2))) unsigned u32x2;
typedef __attribute__((address_space(3))) void* lds_ptr;
typedef __attribute__((address_space(3))) s16x4* lds_s16x4_ptr;

#define AUX_IN 2
#define AUX_ST1 16
#define AUX_ST2 16
#define AUX_ST3 0
#define XPAD 0
#define WPAD 0
#define YPAD 0

constexpr int T_LEN = 4000, L_SEQ = 2000;
constexpr unsigned XF = 262144 + XPAD;
constexpr unsigned WF = 524288 + WPAD;
constexpr unsigned YT = 65536 + YPAD;
constexpr unsigned X_OFF = 0;
constexpr unsigned W_OFF = 16u << 20;
constexpr unsigned Y_OFF = 48u << 20;
constexpr unsigned O_OFF = 80u << 20;
constexpr unsigned RS3 = 0x00020000;

#define STAMPS
__device__ unsigned long long g_st[4 * 8192];
#define STAMP(slot, which) do { if (threadIdx.x == 0) g_st[(slot) * 8192 + blockIdx.x * 2 + (which)] = wall_clock64(); } while (0)
#define STAMP_END(slot) do { asm volatile("s_waitcnt vmcnt(0)" ::: "memory"); __syncthreads(); STAMP(slot, 1); } while (0)

__device__ __forceinline__ unsigned pk(float a, float b) {
    bf16x2 r = {(__bf16)a, (__bf16)b};
    return __builtin_bit_cast(unsigned, r);
}
__device__ __forceinline__ float bf_lo(unsigned u) { return __builtin_bit_cast(float, u << 16); }
__device__ __forceinline__ float bf_hi(unsigned u) { return __builtin_bit_cast(float, u & 0xffff0000u); }
__device__ __forceinline__ f32x16 mfma(u32x4 a, u32x4 b, f32x16 c) {
    return __builtin_amdgcn_mfma_f32_32x32x16_bf16(__builtin_bit_cast(bf16x8, a), __builtin_bit_cast(bf16x8, b), c, 0, 0, 0);
}
__device__ __forceinline__ u32x2 tr_read(const char* p) {
    return __builtin_bit_cast(u32x2, __builtin_amdgcn_ds_read_tr16_b64_v4i16((lds_s16x4_ptr)p));
}
__device__ __forceinline__ void cos_pair(int i0, int i1, float scale, unsigned& hi, unsigned& lo) {
    float c0 = __builtin_amdgcn_cosf((float)(i0 & 127) * (1.0f / 128.0f)) * scale;
    float c1 = __builtin_amdgcn_cosf((float)(i1 & 127) * (1.0f / 128.0f)) * scale;
    hi = pk(c0, c1);
    lo = pk(c0 - bf_lo(hi), c1 - bf_hi(hi));
}

#define CODE_PREFETCH(SYM, N) do { \
    unsigned lo_, hi_; \
    asm volatile("s_getpc_b64 s[20:21]\n\ts_add_u32 s20, s20, " #SYM "@rel32@lo+4\n\ts_addc_u32 s21, s21, " #SYM "@rel32@hi+12\n\ts_mov_b32 %0, s20\n\ts_mov_b32 %1, s21" : "=s"(lo_), "=s"(hi_) :: "s20", "s21", "scc"); \
    const char* p_ = (const char*)(((unsigned long long)hi_ << 32) | lo_); \
    _Pragma("unroll") for (int i_ = 0; i_ < (N); ++i_) asm volatile("global_load_dword %0, %1, %2" : "=v"(pfv[i_]) : "v"((threadIdx.x & 63) * 128 + i_ * 8192), "s"(p_) : "memory"); \
} while (0)
constexpr int NA = 128, NW = 256, NO = 32;
extern "C" __global__ __launch_bounds__(256, 2) void k1_forward(const char* __restrict__ x, const char* __restrict__ w,
                                                      const unsigned short* __restrict__ wo, char* __restrict__ ws) {
    __shared__ __attribute__((aligned(16))) char lds[36864];
    STAMP(0, 0);
    unsigned pfv[3] = {0, 0, 0};
    if (blockIdx.x < 32 && threadIdx.x < 64) CODE_PREFETCH(k1_forward, 3);
    const int bid = blockIdx.x, tid = threadIdx.x, lane = tid & 63;
    const int wv = __builtin_amdgcn_readfirstlane(tid >> 6);
    auto rs_ws = __builtin_amdgcn_make_buffer_rsrc((void*)ws, 0, 0x7fffffff, RS3);
    const int hh = lane >> 5, r32 = lane & 31;
    const int half = wv & 1, rt = wv >> 1;
    const int g4 = lane >> 4, q = (lane >> 2) & 3, p = lane & 3;
    const char* trb = lds + (8 * (g4 >> 1) + q) * 128 + ((64 * half + 32 * (g4 & 1) + 8 * p) ^ (64 * (q >> 1)));
    const int srow = lane >> 3, schunk = (lane & 7) ^ (4 * ((srow >> 1) & 1));
    const int fr = 2 * (16 * rt + (r32 >> 1)) + 1, ph = 32 * (r32 & 1);
    if (bid < NA) {
        const int oc = bid >> 2, cq = bid & 3, s = oc >> 3, o = oc & 7;
        auto rs = __builtin_amdgcn_make_buffer_rsrc((void*)x, 0, 2 * T_LEN * 512, RS3);
#pragma unroll
        for (int i = 0; i < 9; ++i) {
            const int u = wv * 9 + i;
            const int m = 256 * o - 32 + 8 * u + srow;
            const int off = (m >= 0 && m < L_SEQ) ? ((s >> 1) * T_LEN + 2 * m + (s & 1)) * 512 + cq * 128 + schunk * 16 : 0x7ffffff0;
            __builtin_amdgcn_raw_ptr_buffer_load_lds(rs, (lds_ptr)(lds + u * 1024), 16, off, 0, 0, AUX_IN);
        }
        u32x4 fh[4], fl[4];
#pragma unroll
        for (int ks = 0; ks < 4; ++ks)
#pragma unroll
            for (int j = 0; j < 4; ++j) {
                const int n = 16 * ks + 8 * hh + 2 * j;
                unsigned a, b; cos_pair(fr * n + ph, fr * (n + 1) + ph, 1.0f, a, b); fh[ks][j] = a; fl[ks][j] = b;
            }
        __syncthreads();
        f32x16 acc[8];
#pragma unroll
        for (int j = 0; j < 8; ++j) acc[j] = (f32x16)(0.0f);
#pragma unroll
        for (int b = 0; b < 9; ++b)
#pragma unroll
            for (int k2 = 0; k2 < 2; ++k2) {
                u32x2 v0 = tr_read(trb + b * 4096 + k2 * 2048), v1 = tr_read(trb + b * 4096 + k2 * 2048 + 512);
                u32x4 v = {v0.x, v0.y, v1.x, v1.y};
                if (b < 8) { acc[b] = mfma(fh[k2], v, acc[b]); acc[b] = mfma(fl[k2], v, acc[b]); }
                if (b > 0) { acc[b - 1] = mfma(fh[2 + k2], v, acc[b - 1]); acc[b - 1] = mfma(fl[2 + k2], v, acc[b - 1]); }
            }
        const int c = 64 * cq + 32 * half + r32, og = o & 3;
        const unsigned voff = X_OFF + (16 * rt + 2 * hh) * XF + (2 * s + (o >> 2)) * 32768 + (c >> 4) * 2048 + og * 256 + (((c & 15) ^ (4 * og)) * 16);
#pragma unroll
        for (int e = 0; e < 16; ++e) {
            u32x4 v = {pk(acc[0][e], acc[1][e]), pk(acc[2][e], acc[3][e]), pk(acc[4][e], acc[5][e]), pk(acc[6][e], acc[7][e])};
            __builtin_amdgcn_raw_buffer_store_b128(v, rs_ws, voff, (((e & 3) >> 1) + 4 * (e >> 2)) * XF + (e & 1) * 1024, AUX_ST1);
        }
    } else if (bid < NA + NW) {
        const int wb = bid - NA, oct = wb >> 3, dg = wb & 7;
        auto rs = __builtin_amdgcn_make_buffer_rsrc((void*)w, 0, 31 * 256 * 1024, RS3);
#pragma unroll
        for (int i = 0; i < 8; ++i) {
            const int cc = wv * 2 + (i >> 2), tap = 8 * (i & 3) + srow;
            const int off = (tap * 256 + 8 * oct + cc) * 1024 + dg * 128 + schunk * 16;
            __builtin_amdgcn_raw_ptr_buffer_load_lds(rs, (lds_ptr)(lds + (wv * 8 + i) * 1024), 16, off, 0, 0, AUX_IN);
        }
        u32x4 fh[2], fl[2];
#pragma unroll
        for (int ks = 0; ks < 2; ++ks)
#pragma unroll
            for (int j = 0; j < 4; ++j) {
                const int n = 16 * ks + 8 * hh + 2 * j;
                unsigned a, b; cos_pair(fr * n + ph, fr * (n + 1) + ph, 1.0f, a, b); fh[ks][j] = a; fl[ks][j] = b;
            }
        __syncthreads();
        f32x16 acc[8];
#pragma unroll
        for (int j = 0; j < 8; ++j) {
            acc[j] = (f32x16)(0.0f);
#pragma unroll
            for (int k2 = 0; k2 < 2; ++k2) {
                u32x2 v0 = tr_read(trb + j * 4096 + k2 * 2048), v1 = tr_read(trb + j * 4096 + k2 * 2048 + 512);
                u32x4 v = {v0.x, v0.y, v1.x, v1.y};
                acc[j] = mfma(fh[k2], v, acc[j]); acc[j] = mfma(fl[k2], v, acc[j]);
            }
        }
        const unsigned voff = W_OFF + (16 * rt + 2 * hh) * WF + (2 * dg + half) * 32768 + (oct >> 1) * 2048 + (oct & 1) * 512 + r32 * 16;
#pragma unroll
        for (int e = 0; e < 16; ++e) {
            u32x4 v = {pk(acc[0][e], acc[1][e]), pk(acc[2][e], acc[3][e]), pk(acc[4][e], acc[5][e]), pk(acc[6][e], acc[7][e])};
            __builtin_amdgcn_raw_buffer_store_b128(v, rs_ws, voff, (((e & 3) >> 1) + 4 * (e >> 2)) * WF + (e & 1) * 1024, AUX_ST1);
        }
    } else {
        const int piece = (bid - NA - NW) * 4 + wv;
        const int eg = piece >> 4, kz = piece & 15;
        unsigned v[8];
#pragma unroll
        for (int j = 0; j < 8; ++j) v[j] = wo[(16 * kz + 8 * (j >> 2) + 4 * hh + (j & 3)) * 256 + 32 * eg + r32];
        u32x4 o4 = {v[0] | (v[1] << 16), v[2] | (v[3] << 16), v[4] | (v[5] << 16), v[6] | (v[7] << 16)};
        __builtin_amdgcn_raw_buffer_store_b128(o4, rs_ws, O_OFF + piece * 1024 + lane * 16, 0, AUX_ST1);
    }
    asm volatile("s_waitcnt vmcnt(0)" ::: "memory");
    asm volatile("" :: "v"(pfv[0]), "v"(pfv[1]), "v"(pfv[2]));
    STAMP_END(0);
}

__device__ __forceinline__ void glds16(const char* sbase, unsigned voff, unsigned ldsaddr) {
    unsigned keep;
    asm volatile("s_mov_b32 %0, m0\n\ts_mov_b32 m0, %3\n\ts_nop 0\n\tglobal_load_lds_dwordx4 %1, %2\n\ts_mov_b32 m0, %0"
                 : "=&s"(keep) : "v"(voff), "s"(sbase), "s"(ldsaddr) : "memory");
}
template <int OFF>
__device__ __forceinline__ void gload16(u32x4& d, const char* sbase, unsigned voff) {
    asm volatile("global_load_dwordx4 %0, %1, %2 offset:%3" : "=v"(d) : "v"(voff), "s"(sbase), "i"(OFF) : "memory");
}
#define WAIT_VM(n) asm volatile("s_waitcnt vmcnt(" #n ")" ::: "memory")
#define PIN8(a) asm volatile("" : "+v"((a)[0]), "+v"((a)[1]), "+v"((a)[2]), "+v"((a)[3]), "+v"((a)[4]), "+v"((a)[5]), "+v"((a)[6]), "+v"((a)[7]))

extern "C" __global__ __launch_bounds__(256, 1) void k2_gemm(char* __restrict__ ws) {
    extern __shared__ __attribute__((aligned(16))) char lds[];
    STAMP(1, 0);
    const int bid = blockIdx.x, tid = threadIdx.x, lane = tid & 63;
    const int wv = __builtin_amdgcn_readfirstlane(tid >> 6);
    const int f = (bid & 7) * 4 + (bid >> 6), sub = (bid >> 3) & 7, th = sub >> 2, cg = (sub & 3) * 4 + wv;
    const char* wsrc = ws + W_OFF + f * WF + cg * 32768;
    const char* xsrc = ws + X_OFF + f * XF + th * 131072;
    const unsigned l16 = lane * 16;
    const unsigned ldsb = (unsigned)(size_t)(lds_ptr)lds;
    u32x4 wf[32];
#pragma unroll
    for (int kq = 0; kq < 4; ++kq) {
        const char* b = wsrc + kq * 8192 + 4096;
        gload16<-4096>(wf[kq * 8 + 0], b, l16); gload16<-3072>(wf[kq * 8 + 1], b, l16);
        gload16<-2048>(wf[kq * 8 + 2], b, l16); gload16<-1024>(wf[kq * 8 + 3], b, l16);
        gload16<0>(wf[kq * 8 + 4], b, l16); gload16<1024>(wf[kq * 8 + 5], b, l16);
        gload16<2048>(wf[kq * 8 + 6], b, l16); gload16<3072>(wf[kq * 8 + 7], b, l16);
#pragma unroll
        for (int i = 0; i < 2; ++i) { const int o = kq * 8192 + (wv * 2 + i) * 1024; glds16(xsrc + o, l16, ldsb + o); }
    }
#pragma unroll
    for (int g = 1; g < 3; ++g)
#pragma unroll
        for (int i = 0; i < 8; ++i) { const int o = g * 32768 + (wv * 8 + i) * 1024; glds16(xsrc + o, l16, ldsb + o); }

    const int g4 = lane >> 4, q = (lane >> 2) & 3, p = lane & 3;
    const int og = 2 * (g4 & 1) + (p >> 1);
    const int ta = og * 256 + (((8 * (g4 >> 1) + q) ^ (4 * og)) * 16) + 8 * (p & 1);
    const char* t0 = lds + ta;
    const char* t1 = lds + (ta ^ 64);
    auto rs_ws = __builtin_amdgcn_make_buffer_rsrc((void*)ws, 0, 0x7fffffff, RS3);
    const int hh = lane >> 5, r32 = lane & 31;
    const unsigned voff = Y_OFF + (128 * th + 4 * hh) * YT + f * 2048 + (32 * cg + r32) * 4;

    f32x16 p1[4], p2[4], p3[4];
#pragma unroll
    for (int g = 0; g < 4; ++g) {
        p1[g] = (f32x16)(0.0f); p2[g] = (f32x16)(0.0f); p3[g] = (f32x16)(0.0f);
#pragma unroll
        for (int kq = 0; kq < 4; ++kq) {
            if (g == 0) {
                if (kq == 0) WAIT_VM(46);
                if (kq == 1) WAIT_VM(44);
                if (kq == 2) WAIT_VM(34);
                if (kq == 3) WAIT_VM(24);
                PIN8(&wf[kq * 8]);
                __builtin_amdgcn_s_barrier();
                asm volatile("" ::: "memory");
                if (kq == 0) {
#pragma unroll
                    for (int i = 0; i < 8; ++i) { const int o = 3 * 32768 + (wv * 8 + i) * 1024; glds16(xsrc + o, l16, ldsb + o); }
                }
            } else if (kq == 0) {
                if (g == 1) WAIT_VM(16);
                if (g == 2) WAIT_VM(24);
                if (g == 3) WAIT_VM(32);
                __builtin_amdgcn_s_barrier();
                asm volatile("" ::: "memory");
            }
#pragma unroll
            for (int k4 = 0; k4 < 4; ++k4) {
                const int ks = kq * 4 + k4, o = g * 32768 + ks * 2048;
                u32x2 a0 = tr_read(t0 + o), a1 = tr_read(t1 + o), b0 = tr_read(t0 + o + 1024), b1 = tr_read(t1 + o + 1024);
                u32x4 xr = {a0.x, a0.y, a1.x, a1.y}, xi = {b0.x, b0.y, b1.x, b1.y};
                p1[g] = mfma(xr, wf[2 * ks], p1[g]);
                p3[g] = mfma(xr, wf[2 * ks + 1], p3[g]);
                p2[g] = mfma(xi, wf[2 * ks + 1], p2[g]);
                p3[g] = mfma(xi, wf[2 * ks], p3[g]);
            }
            if (g > 0) {
#pragma unroll
                for (int e = 4 * kq; e < 4 * kq + 4; ++e) {
                    const unsigned v = pk(p1[g - 1][e] - p2[g - 1][e], p3[g - 1][e]);
                    __builtin_amdgcn_raw_buffer_store_b32(v, rs_ws, voff, (32 * (g - 1) + (e & 3) + 8 * (e >> 2)) * YT, AUX_ST2);
                }
            }
        }
    }
#pragma unroll
    for (int e = 0; e < 16; ++e) {
        const unsigned v = pk(p1[3][e] - p2[3][e], p3[3][e]);
        __builtin_amdgcn_raw_buffer_store_b32(v, rs_ws, voff, (96 + (e & 3) + 8 * (e >> 2)) * YT, AUX_ST2);
    }
    STAMP_END(1);
}

extern "C" __global__ __launch_bounds__(512, 2) void k3_inverse(const char* __restrict__ ws, char* __restrict__ out) {
    __shared__ __attribute__((aligned(16))) char zb[16384];
    STAMP(2, 0);
    const int bid = blockIdx.x, tid = threadIdx.x, lane = tid & 63;
    const int wv = __builtin_amdgcn_readfirstlane(tid >> 6);
    const int s = bid / 63, k = bid - 63 * s, tile = 64 * s + k;
    const int hh = lane >> 5, r32 = lane & 31;
    auto rs_ws = __builtin_amdgcn_make_buffer_rsrc((void*)ws, 0, 0x7fffffff, RS3);
    const unsigned voff = Y_OFF + tile * YT + hh * 8192 + (32 * wv + r32) * 4;
    u32x4 yf[4], yg[4];
#pragma unroll
    for (int ks = 0; ks < 4; ++ks)
#pragma unroll
        for (int j = 0; j < 4; ++j) {
            yf[ks][j] = __builtin_amdgcn_raw_buffer_load_b32(rs_ws, voff, ks * 16384 + j * 2048, 0);
            yg[ks][j] = __builtin_amdgcn_raw_buffer_load_b32(rs_ws, voff + 1024, ks * 16384 + j * 2048, 0);
        }
    u32x4 wo[16];
#pragma unroll
    for (int kz = 0; kz < 16; ++kz) wo[kz] = __builtin_amdgcn_raw_buffer_load_b128(rs_ws, O_OFF + wv * 16384 + lane * 16, kz * 1024, 0);
    u32x4 gh[4], gl[4];
#pragma unroll
    for (int ks = 0; ks < 4; ++ks)
#pragma unroll
        for (int j = 0; j < 4; ++j) {
            const int i0 = (2 * (8 * ks + 4 * hh + j) + 1) * (32 + r32);
            unsigned a, b; cos_pair(i0, i0 + 32, 1.0f / 32.0f, a, b); gh[ks][j] = a; gl[ks][j] = b;
        }
    f32x16 hf = (f32x16)(0.0f), hg = (f32x16)(0.0f);
#pragma unroll
    for (int ks = 0; ks < 4; ++ks) {
        hf = mfma(yf[ks], gh[ks], hf); hg = mfma(yg[ks], gh[ks], hg);
        hf = mfma(yf[ks], gl[ks], hf); hg = mfma(yg[ks], gl[ks], hg);
    }
    float z[16];
#pragma unroll
    for (int e = 0; e < 16; ++e) {
        const float a = __builtin_amdgcn_exp2f(__builtin_amdgcn_fmed3f(hf[e] * 2.8853900817779268f, -60.0f, 60.0f));
        const float b = __builtin_amdgcn_exp2f(hg[e] * -1.4426950408889634f);
        z[e] = (a - 1.0f) * __builtin_amdgcn_rcpf((a + 1.0f) * (1.0f + b));
    }
#pragma unroll
    for (int sg = 0; sg < 2; ++sg) {
        u32x4 v = {pk(z[8 * sg], z[8 * sg + 1]), pk(z[8 * sg + 2], z[8 * sg + 3]), pk(z[8 * sg + 4], z[8 * sg + 5]), pk(z[8 * sg + 6], z[8 * sg + 7])};
        *(u32x4*)(zb + (2 * wv + sg) * 1024 + lane * 16) = v;
    }
    __syncthreads();
    f32x16 o = (f32x16)(0.0f);
#pragma unroll
    for (int kz = 0; kz < 16; ++kz) o = mfma(wo[kz], *(const u32x4*)(zb + kz * 1024 + lane * 16), o);
    const int m = 32 * k + r32;
    auto rs_o = __builtin_amdgcn_make_buffer_rsrc((void*)out, 0, 2 * T_LEN * 512, RS3);
    const unsigned ooff = m < L_SEQ ? ((s >> 1) * T_LEN + 2 * m + (s & 1)) * 512 + 64 * wv + 16 * hh : 0x7ffffff0;
#pragma unroll
    for (int mm = 0; mm < 4; mm += 2) {
        unsigned a0 = pk(o[4 * mm], o[4 * mm + 1]), a1 = pk(o[4 * mm + 2], o[4 * mm + 3]);
        unsigned b0 = pk(o[4 * mm + 4], o[4 * mm + 5]), b1 = pk(o[4 * mm + 6], o[4 * mm + 7]);
        auto r0 = __builtin_amdgcn_permlane32_swap(a0, b0, false, false);
        auto r1 = __builtin_amdgcn_permlane32_swap(a1, b1, false, false);
        u32x4 v = {r0[0], r1[0], r0[1], r1[1]};
        __builtin_amdgcn_raw_buffer_store_b128(v, rs_o, ooff, mm * 16, AUX_ST3);
    }
    STAMP_END(2);
}

extern "C" __global__ void k4_diag(char* ws) {
    __shared__ unsigned long long sm[6][256];
    const int g[3] = {416, 256, 252};
    for (int k = 0; k < 3; ++k) {
        unsigned long long mn = ~0ull, mx = 0;
        for (int b = threadIdx.x; b < g[k]; b += 256) { unsigned long long a = g_st[k * 8192 + 2 * b], e = g_st[k * 8192 + 2 * b + 1]; mn = a < mn ? a : mn; mx = e > mx ? e : mx; }
        sm[2 * k][threadIdx.x] = mn; sm[2 * k + 1][threadIdx.x] = mx;
    }
    __syncthreads();
    if (threadIdx.x == 0) {
        unsigned long long t[6];
        for (int k = 0; k < 6; ++k) { unsigned long long v = sm[k][0]; for (int i = 1; i < 256; ++i) { unsigned long long u = sm[k][i]; v = (k & 1) ? (u > v ? u : v) : (u < v ? u : v); } t[k] = v; }
        unsigned long long q = QSEL == 0 ? 0 : t[QSEL] - t[QSEL - 1];
        if (q > 10000) q = 10000;
        unsigned long long t0 = wall_clock64();
        while (wall_clock64() - t0 < 10 * q) __builtin_amdgcn_s_sleep(1);
    }
}
extern "C" void kernel_launch(void* const* d_in, const int* in_sizes, int n_in,
                              void* d_out, int out_size, void* d_ws, size_t ws_size,
                              hipStream_t stream) {
    (void)hipFuncSetAttribute((const void*)k2_gemm, hipFuncAttributeMaxDynamicSharedMemorySize, 131072);
    k1_forward<<<NA + NW + NO, 256, 0, stream>>>((const char*)d_in[0], (const char*)d_in[1], (const unsigned short*)d_in[2], (char*)d_ws);
    k2_gemm<<<256, 256, 131072, stream>>>((char*)d_ws);
    k3_inverse<<<252, 512, 0, stream>>>((const char*)d_ws, (char*)d_out);
    k4_diag<<<1, 256, 0, stream>>>((char*)d_ws);
}
```

```cpp
#include <hip/hip_runtime.h>
#include <hip/hip_bf16.h>
#include <utility>

typedef __attribute__((ext_vector_type(8))) __bf16 bf16x8;
typedef __attribute__((ext_vector_type(2))) __bf16 bf16x2;
typedef __attribute__((ext_vector_type(4))) short s16x4;
typedef __attribute__((ext_vector_type(16))) float f32x16;
typedef __attribute__((ext_vector_type(4))) float f32x4;
typedef __attribute__((ext_vector_type(4))) unsigned u32x4;
typedef __attribute__((ext_vector_type(2))) unsigned u32x2;
typedef __attribute__((address_space(3))) void* lds_ptr;
typedef __attribute__((address_space(3))) s16x4* lds_s16x4_ptr;
#define DI __device__ __forceinline__
#define GAP() __builtin_amdgcn_sched_barrier(0)
template <class F, int... Is> DI void static_for_impl(F&& f, std::integer_sequence<int, Is...>) { (f(std::integral_constant<int, Is>{}), ...); }
template <int N, class F> DI void static_for(F&& f) { static_for_impl(f, std::make_integer_sequence<int, N>{}); }

#define AUX_IN 2
#define AUX_ST 16
#define AUX_ST3 0

constexpr int T_LEN = 4000, L_SEQ = 2000;
constexpr unsigned XF = 262144;
constexpr unsigned WF = 524288;
constexpr unsigned YT = 65536;
constexpr unsigned X_OFF = 0;
constexpr unsigned W_OFF = 16u << 20;
constexpr unsigned Y_OFF = 48u << 20;
constexpr unsigned O_OFF = 80u << 20;
constexpr unsigned RS3 = 0x00020000;

#define STAMP(slot, which)
#define STAMP_END(slot)
#define FINE_DECL
#define FINE(k, i)
#define FINE_OUT(k, n)

DI unsigned pk(float a, float b) {
    bf16x2 r = {(__bf16)a, (__bf16)b};
    return __builtin_bit_cast(unsigned, r);
}
DI float bf_lo(unsigned u) { return __builtin_bit_cast(float, u << 16); }
DI float bf_hi(unsigned u) { return __builtin_bit_cast(float, u & 0xffff0000u); }
DI f32x16 mfma32(u32x4 a, u32x4 b, f32x16 c) {
    return __builtin_amdgcn_mfma_f32_32x32x16_bf16(__builtin_bit_cast(bf16x8, a), __builtin_bit_cast(bf16x8, b), c, 0, 0, 0);
}
DI f32x4 mfma16(u32x4 a, u32x4 b, f32x4 c) {
    return __builtin_amdgcn_mfma_f32_16x16x32_bf16(__builtin_bit_cast(bf16x8, a), __builtin_bit_cast(bf16x8, b), c, 0, 0, 0);
}
DI u32x2 tr_read(const char* p) {
    return __builtin_bit_cast(u32x2, __builtin_amdgcn_ds_read_tr16_b64_v4i16((lds_s16x4_ptr)p));
}
DI void cos_pair(int i0, int i1, float scale, unsigned& hi, unsigned& lo) {
    float c0 = __builtin_amdgcn_cosf((float)(i0 & 127) * (1.0f / 128.0f)) * scale;
    float c1 = __builtin_amdgcn_cosf((float)(i1 & 127) * (1.0f / 128.0f)) * scale;
    hi = pk(c0, c1);
    lo = pk(c0 - bf_lo(hi), c1 - bf_hi(hi));
}
#define WAIT_VM(n) asm volatile("s_waitcnt vmcnt(" #n ")" ::: "memory")
#define BARRIER() do { __builtin_amdgcn_s_barrier(); asm volatile("" ::: "memory"); } while (0)
DI void blds16(u32x4 rs, unsigned voff, unsigned ldsaddr) {
    asm volatile("s_mov_b32 m0, %2\n\ts_nop 0\n\tbuffer_load_dwordx4 %0, %1, 0 offen nt lds" :: "v"(voff), "s"(rs), "s"(ldsaddr) : "memory");
}
template <int OFF>
DI void glds16(const char* sbase, unsigned voff, unsigned ldsaddr) {
    asm volatile("s_mov_b32 m0, %2\n\ts_nop 0\n\tglobal_load_lds_dwordx4 %0, %1 offset:%3 sc1" :: "v"(voff), "s"(sbase), "s"(ldsaddr), "i"(OFF) : "memory");
}
template <int OFF>
DI void gload16(u32x4& d, const char* sbase, unsigned voff) {
    asm volatile("global_load_dwordx4 %0, %1, %2 offset:%3 sc1" : "=v"(d) : "v"(voff), "s"(sbase), "i"(OFF) : "memory");
}
DI u32x4 make_rs(const void* p, unsigned bytes) {
    const unsigned long long a = (unsigned long long)p;
    u32x4 r = {(unsigned)a, (unsigned)(a >> 32) & 0xffffu, bytes, RS3};
    return r;
}
#define PIN8(a) asm volatile("" : "+v"((a)[0]), "+v"((a)[1]), "+v"((a)[2]), "+v"((a)[3]), "+v"((a)[4]), "+v"((a)[5]), "+v"((a)[6]), "+v"((a)[7]))

#define CODE_PREFETCH(SYM, N) do { \
    unsigned lo_, hi_; \
    asm volatile("s_getpc_b64 s[20:21]\n\ts_add_u32 s20, s20, " #SYM "@rel32@lo+4\n\ts_addc_u32 s21, s21, " #SYM "@rel32@hi+12\n\ts_mov_b32 %0, s20\n\ts_mov_b32 %1, s21" : "=s"(lo_), "=s"(hi_) :: "s20", "s21", "scc"); \
    const char* p_ = (const char*)(((unsigned long long)hi_ << 32) | lo_); \
    _Pragma("unroll") for (int i_ = 0; i_ < (N); ++i_) asm volatile("global_load_dword %0, %1, %2" : "=v"(pfv[i_]) : "v"((threadIdx.x & 63) * 128 + i_ * 8192), "s"(p_) : "memory"); \
} while (0)

DI void w_task(const char* t0, const char* t1, const u32x4& fh, const u32x4& fl, decltype(__builtin_amdgcn_make_buffer_rsrc((void*)0, 0, 0, 0)) rs_ws, unsigned voff) {
    f32x4 acc[8][2];
#pragma unroll
    for (int j = 0; j < 8; ++j)
#pragma unroll
        for (int ct = 0; ct < 2; ++ct) {
            const char* t = (ct ? t1 : t0) + j * 4096;
            u32x2 v0 = tr_read(t), v1 = tr_read(t + 512);
            u32x4 v = {v0.x, v0.y, v1.x, v1.y};
            acc[j][ct] = mfma16(fh, v, (f32x4)(0.0f));
            acc[j][ct] = mfma16(fl, v, acc[j][ct]);
        }
#pragma unroll
    for (int ct = 0; ct < 2; ++ct)
#pragma unroll
        for (int i = 0; i < 4; ++i) {
            u32x4 v = {pk(acc[0][ct][i], acc[1][ct][i]), pk(acc[2][ct][i], acc[3][ct][i]), pk(acc[4][ct][i], acc[5][ct][i]), pk(acc[6][ct][i], acc[7][ct][i])};
            __builtin_amdgcn_raw_buffer_store_b128(v, rs_ws, voff + (i & 1) * 1024 + ct * 256, (i >> 1) * WF, AUX_ST);
        }
}

extern "C" __global__ __launch_bounds__(256, 2) void k1_forward(const char* __restrict__ x, const char* __restrict__ w,
                                                      const unsigned short* __restrict__ wo, char* __restrict__ ws) {
    extern __shared__ __attribute__((aligned(16))) char lds[];
    STAMP(0, 0);
    FINE_DECL;
    FINE(1, 0);
    unsigned pfv[3] = {0, 0, 0};
    const int bid = blockIdx.x, tid = threadIdx.x, lane = tid & 63;
    const int wv = __builtin_amdgcn_readfirstlane(tid >> 6);
    if (bid < 32 && wv == 3) CODE_PREFETCH(k1_forward, 3);
    const unsigned ldsb = (unsigned)(size_t)(lds_ptr)lds;
    const int fhalf = (bid >> 3) & 1, tk = (bid & 7) + 8 * (bid >> 4);
    const int srow = lane >> 3, schunk = (lane & 7) ^ (srow & 2);
    const int oc = tk >> 2, cq4 = tk & 3, s = oc >> 3, o = oc & 7;
    {
        const u32x4 rs = make_rs(x, 2 * T_LEN * 512);
#pragma unroll
        for (int i = 0; i < 9; ++i) {
            const int u = wv * 9 + i;
            const int m = 256 * o - 32 + 8 * u + srow;
            const unsigned off = ((unsigned)m < (unsigned)L_SEQ) ? ((s >> 1) * T_LEN + 2 * m + (s & 1)) * 512 + cq4 * 128 + (schunk ^ ((u & 1) << 2)) * 16 : 0x7ffffff0u;
            blds16(rs, off, ldsb + u * 1024);
        }
    }
    const int oct0 = tk >> 3, dg = tk & 7;
    {
        const u32x4 rs = make_rs(w, 31 * 256 * 1024);
#pragma unroll
        for (int hf = 0; hf < 2; ++hf)
#pragma unroll
            for (int i = 0; i < 8; ++i) {
                const int cc = wv * 2 + (i >> 2), tap = 8 * (i & 3) + srow;
                const unsigned off = (tap * 256 + 8 * (oct0 + 16 * hf) + cc) * 1024 + dg * 128 + (schunk ^ ((i & 1) << 2)) * 16;
                blds16(rs, off, ldsb + 36864 + hf * 32768 + (wv * 8 + i) * 1024);
            }
    }
    FINE(1, 1);
    auto rs_ws = __builtin_amdgcn_make_buffer_rsrc((void*)ws, 0, 0x7fffffff, RS3);
    const int g4 = lane >> 4, q = (lane >> 2) & 3, p = lane & 3, col = lane & 15;
    const int ch = wv & 1, fq = wv >> 1;
    const int ta = (8 * g4 + q) * 128 + (((2 * ch) ^ ((q >> 1) | ((g4 & 1) << 1))) * 32) + 8 * p;
    const char* t0 = lds + ta;
    const char* t1 = lds + (ta ^ 32);
    u32x4 fh[2], fl[2];
    {
        const int rho = 32 * fhalf + 16 * fq + col, fr = (rho | 1), ph = 32 * (rho & 1);
#pragma unroll
        for (int kb = 0; kb < 2; ++kb)
#pragma unroll
            for (int j = 0; j < 4; ++j) {
                const int n = 32 * kb + 8 * g4 + 2 * j;
                unsigned a, b; cos_pair(fr * n + ph, fr * (n + 1) + ph, 1.0f, a, b); fh[kb][j] = a; fl[kb][j] = b;
            }
    }
    const int f0 = 16 * fhalf + 8 * fq + 2 * g4;
    FINE(1, 2);
    WAIT_VM(16); BARRIER();
    FINE(1, 3);
    {
        f32x4 acc[8][2];
#pragma unroll
        for (int b = 0; b < 9; ++b)
#pragma unroll
            for (int ct = 0; ct < 2; ++ct) {
                const char* t = (ct ? t1 : t0) + b * 4096;
                u32x2 v0 = tr_read(t), v1 = tr_read(t + 512);
                u32x4 v = {v0.x, v0.y, v1.x, v1.y};
                if (b > 0) { acc[b - 1][ct] = mfma16(fh[1], v, acc[b - 1][ct]); acc[b - 1][ct] = mfma16(fl[1], v, acc[b - 1][ct]); }
                if (b < 8) { acc[b][ct] = mfma16(fh[0], v, (f32x4)(0.0f)); acc[b][ct] = mfma16(fl[0], v, acc[b][ct]); }
            }
        const int og = o & 3;
        const unsigned voff = X_OFF + f0 * XF + (2 * s + (o >> 2)) * 32768 + (4 * cq4 + 2 * ch) * 2048 + og * 256 + ((col ^ (4 * og)) * 16);
#pragma unroll
        for (int ct = 0; ct < 2; ++ct)
#pragma unroll
            for (int i = 0; i < 4; ++i) {
                u32x4 v = {pk(acc[0][ct][i], acc[1][ct][i]), pk(acc[2][ct][i], acc[3][ct][i]), pk(acc[4][ct][i], acc[5][ct][i]), pk(acc[6][ct][i], acc[7][ct][i])};
                __builtin_amdgcn_raw_buffer_store_b128(v, rs_ws, voff + (i & 1) * 1024 + ct * 2048, (i >> 1) * XF, AUX_ST);
            }
    }
    FINE(1, 4);
    const unsigned wvoff = W_OFF + f0 * WF + (2 * dg + ch) * 32768 + (oct0 >> 1) * 2048 + (oct0 & 1) * 512 + col * 16;
    WAIT_VM(16); BARRIER();
    FINE(1, 5);
    w_task(t0 + 36864, t1 + 36864, fh[0], fl[0], rs_ws, wvoff);
    FINE(1, 6);
    WAIT_VM(16); BARRIER();
    FINE(1, 7);
    w_task(t0 + 69632, t1 + 69632, fh[0], fl[0], rs_ws, wvoff + 16384);
    FINE(1, 8);
    if (bid < 128 && wv == 0) {
        const int eg = bid >> 4, kz = bid & 15, hh = lane >> 5, r32 = lane & 31;
        unsigned v[8];
#pragma unroll
        for (int j = 0; j < 8; ++j) v[j] = wo[(16 * kz + 8 * (j >> 2) + 4 * hh + (j & 3)) * 256 + 32 * eg + r32];
        u32x4 o4 = {v[0] | (v[1] << 16), v[2] | (v[3] << 16), v[4] | (v[5] << 16), v[6] | (v[7] << 16)};
        __builtin_amdgcn_raw_buffer_store_b128(o4, rs_ws, O_OFF + bid * 1024 + lane * 16, 0, AUX_ST);
    }
    WAIT_VM(0);
    asm volatile("" :: "v"(pfv[0]), "v"(pfv[1]), "v"(pfv[2]));
    FINE(1, 9);
    FINE_OUT(1, 10);
    STAMP_END(0);
}

struct XFrag { u32x2 a0, a1, b0, b1; };
template <int KQ> DI void w_issue(u32x4* wf, const char* wsrc, unsigned l16, int i) {
    const char* b = wsrc + KQ * 8192 + 4096;
    switch (i) {
    case 0: gload16<-4096>(wf[KQ * 8 + 0], b, l16); break;
    case 1: gload16<-3072>(wf[KQ * 8 + 1], b, l16); break;
    case 2: gload16<-2048>(wf[KQ * 8 + 2], b, l16); break;
    case 3: gload16<-1024>(wf[KQ * 8 + 3], b, l16); break;
    case 4: gload16<0>(wf[KQ * 8 + 4], b, l16); break;
    case 5: gload16<1024>(wf[KQ * 8 + 5], b, l16); break;
    case 6: gload16<2048>(wf[KQ * 8 + 6], b, l16); break;
    default: gload16<3072>(wf[KQ * 8 + 7], b, l16); break;
    }
}
DI void x_issue(const char* xsrc, unsigned l16, unsigned ldsb, int o) { glds16<0>(xsrc + o, l16, ldsb + o); }

extern "C" __global__ __launch_bounds__(256, 2) void k2_gemm(char* __restrict__ ws) {
    extern __shared__ __attribute__((aligned(16))) char lds[];
    STAMP(1, 0);
    FINE_DECL;
    FINE(2, 0);
    const int bid = blockIdx.x, tid = threadIdx.x, lane = tid & 63;
    const int wv = __builtin_amdgcn_readfirstlane(tid >> 6);
    const int f = (bid & 7) * 4 + (bid >> 6), sub = (bid >> 3) & 7, th = sub >> 2, cg = (sub & 3) * 4 + wv;
    const char* wsrc = ws + W_OFF + f * WF + cg * 32768;
    const char* xsrc = ws + X_OFF + f * XF + th * 131072;
    const unsigned l16 = lane * 16;
    const unsigned ldsb = (unsigned)(size_t)(lds_ptr)lds;
    u32x4 wf[32];
#pragma unroll
    for (int i = 0; i < 8; ++i) w_issue<0>(wf, wsrc, l16, i);
    x_issue(xsrc, l16, ldsb, wv * 2048); x_issue(xsrc, l16, ldsb, wv * 2048 + 1024);
#pragma unroll
    for (int i = 0; i < 8; ++i) w_issue<1>(wf, wsrc, l16, i);
    x_issue(xsrc, l16, ldsb, 8192 + wv * 2048); x_issue(xsrc, l16, ldsb, 8192 + wv * 2048 + 1024);
#pragma unroll
    for (int i = 0; i < 8; ++i) w_issue<2>(wf, wsrc, l16, i);
    x_issue(xsrc, l16, ldsb, 16384 + wv * 2048); x_issue(xsrc, l16, ldsb, 16384 + wv * 2048 + 1024);
    FINE(2, 1);

    const int g4 = lane >> 4, q = (lane >> 2) & 3, p = lane & 3;
    const int og = 2 * (g4 & 1) + (p >> 1);
    const int ta = og * 256 + (((8 * (g4 >> 1) + q) ^ (4 * og)) * 16) + 8 * (p & 1);
    const char* t0 = lds + ta;
    const char* t1 = lds + (ta ^ 64);
    const char* t2 = t0 + 65536;
    const char* t3 = t1 + 65536;
    auto rs_ws = __builtin_amdgcn_make_buffer_rsrc((void*)ws, 0, 0x7fffffff, RS3);
    const int hh = lane >> 5, r32 = lane & 31;
    const unsigned voff = Y_OFF + (128 * th + 4 * hh) * YT + f * 2048 + (32 * cg + r32) * 4;

    auto rd = [&](XFrag& x, int st, int part) __attribute__((always_inline)) {
        const int o = (st & 31) * 2048;
        const char* a = st < 32 ? t0 : t2;
        const char* b = st < 32 ? t1 : t3;
        if (part == 0) x.a0 = tr_read(a + o);
        if (part == 1) x.a1 = tr_read(b + o);
        if (part == 2) x.b0 = tr_read(a + o + 1024);
        if (part == 3) x.b1 = tr_read(b + o + 1024);
    };
    XFrag xf[3];
    f32x16 yr[2], yi[2];
    WAIT_VM(20); PIN8(&wf[0]); BARRIER();
    FINE(2, 2);
#pragma unroll
    for (int part = 0; part < 4; ++part) rd(xf[0], 0, part);
#pragma unroll
    for (int part = 0; part < 4; ++part) rd(xf[1], 1, part);
    GAP();
    static_for<64>([&](auto ST) __attribute__((always_inline)) {
        constexpr int st = ST.value, g = st >> 4, ks = st & 15;
        if (st == 2)  { WAIT_VM(16); PIN8(&wf[8]);  BARRIER(); FINE(2, 3); }
        if (st == 6)  { WAIT_VM(14); PIN8(&wf[16]); BARRIER(); FINE(2, 4); }
        if (st == 10) { WAIT_VM(12); PIN8(&wf[24]); BARRIER(); FINE(2, 5); }
        if (st == 14) { WAIT_VM(12); BARRIER(); FINE(2, 6); }
        if (st == 30) { WAIT_VM(22); BARRIER(); FINE(2, 7); }
        if (st == 46) { WAIT_VM(30); BARRIER(); FINE(2, 8); }
        const XFrag& x = xf[st % 3];
        XFrag& nx = xf[(st + 2) % 3];
        const u32x4 xr = {x.a0.x, x.a0.y, x.a1.x, x.a1.y}, xi = {x.b0.x, x.b0.y, x.b1.x, x.b1.y};
        const f32x16 zero = (f32x16)(0.0f);
        yr[g & 1] = mfma32(xr, wf[2 * ks], ks ? yr[g & 1] : zero);
        GAP();
        const u32x4 nxi = xi ^ 0x80008000u;
        if (st < 62) rd(nx, st + 2, 0);
        GAP();
        yi[g & 1] = mfma32(xr, wf[2 * ks + 1], ks ? yi[g & 1] : zero);
        GAP();
        if (st < 62) rd(nx, st + 2, 1);
        if (st < 4) { if (st < 3) w_issue<3>(wf, wsrc, l16, st * 3); else x_issue(xsrc, l16, ldsb, 24576 + wv * 2048); }
        else if (st < 16) x_issue(xsrc, l16, ldsb, (st >> 2) * 32768 + (wv * 8 + (st & 3) * 2) * 1024);
        GAP();
        yr[g & 1] = mfma32(nxi, wf[2 * ks + 1], yr[g & 1]);
        GAP();
        if (st < 62) rd(nx, st + 2, 2);
        if (st < 4) { if (st < 3) w_issue<3>(wf, wsrc, l16, st * 3 + 1); else x_issue(xsrc, l16, ldsb, 24576 + wv * 2048 + 1024); }
        else if (st < 16) x_issue(xsrc, l16, ldsb, (st >> 2) * 32768 + (wv * 8 + (st & 3) * 2 + 1) * 1024);
        GAP();
        yi[g & 1] = mfma32(xi, wf[2 * ks], yi[g & 1]);
        GAP();
        if (st < 62) rd(nx, st + 2, 3);
        if (st < 2) w_issue<3>(wf, wsrc, l16, st * 3 + 2);
        if (g > 0) {
            const unsigned v = pk(yr[(g - 1) & 1][ks], yi[(g - 1) & 1][ks]);
            __builtin_amdgcn_raw_buffer_store_b32(v, rs_ws, voff, (32 * (g - 1) + (ks & 3) + 8 * (ks >> 2)) * YT, AUX_ST);
        }
        GAP();
    });
    FINE(2, 9);
#pragma unroll
    for (int e = 0; e < 16; ++e) {
        const unsigned v = pk(yr[1][e], yi[1][e]);
        __builtin_amdgcn_raw_buffer_store_b32(v, rs_ws, voff, (96 + (e & 3) + 8 * (e >> 2)) * YT, AUX_ST);
    }
    FINE(2, 10);
    FINE(2, 11);
    FINE_OUT(2, 12);
    STAMP_END(1);
}

extern "C" __global__ __launch_bounds__(256, 1) void k3_inverse(const char* __restrict__ ws, char* __restrict__ out) {
    __shared__ __attribute__((aligned(16))) char zb[16384];
    STAMP(2, 0);
    FINE_DECL;
    FINE(3, 0);
    const int bid = blockIdx.x, tid = threadIdx.x, lane = tid & 63;
    const int wv = __builtin_amdgcn_readfirstlane(tid >> 6);
    const int s = bid / 63, k = bid - 63 * s, tile = 64 * s + k;
    const int hh = lane >> 5, r32 = lane & 31;
    auto rs_ws = __builtin_amdgcn_make_buffer_rsrc((void*)ws, 0, 0x7fffffff, RS3);
    const unsigned voff = Y_OFF + tile * YT + hh * 8192 + (64 * wv + r32) * 4;
    u32x4 yf[2][4], yg[2][4];
#pragma unroll
    for (int sg = 0; sg < 2; ++sg)
#pragma unroll
        for (int ks = 0; ks < 4; ++ks)
#pragma unroll
            for (int j = 0; j < 4; ++j) {
                yf[sg][ks][j] = __builtin_amdgcn_raw_buffer_load_b32(rs_ws, voff + sg * 128, ks * 16384 + j * 2048, 16);
                yg[sg][ks][j] = __builtin_amdgcn_raw_buffer_load_b32(rs_ws, voff + sg * 128 + 1024, ks * 16384 + j * 2048, 16);
            }
    GAP();
    u32x4 wo[2][16];
#pragma unroll
    for (int et = 0; et < 2; ++et)
#pragma unroll
        for (int kz = 0; kz < 16; ++kz) wo[et][kz] = __builtin_amdgcn_raw_buffer_load_b128(rs_ws, O_OFF + (2 * wv + et) * 16384 + lane * 16, kz * 1024, 16);
    GAP();
    FINE(3, 1);
    u32x4 gh[4], gl[4];
#pragma unroll
    for (int ks = 0; ks < 4; ++ks)
#pragma unroll
        for (int j = 0; j < 4; ++j) {
            const int i0 = (2 * (8 * ks + 4 * hh + j) + 1) * (32 + r32);
            unsigned a, b; cos_pair(i0, i0 + 32, 1.0f / 32.0f, a, b); gh[ks][j] = a; gl[ks][j] = b;
        }
    FINE(3, 2);
#pragma unroll
    for (int sg = 0; sg < 2; ++sg) {
        f32x16 hf = (f32x16)(0.0f), hg = (f32x16)(0.0f);
#pragma unroll
        for (int ks = 0; ks < 4; ++ks) {
            hf = mfma32(yf[sg][ks], gh[ks], hf); hg = mfma32(yg[sg][ks], gh[ks], hg);
            hf = mfma32(yf[sg][ks], gl[ks], hf); hg = mfma32(yg[sg][ks], gl[ks], hg);
        }
        float z[16];
#pragma unroll
        for (int e = 0; e < 16; ++e) {
            const float a = __builtin_amdgcn_exp2f(__builtin_amdgcn_fmed3f(hf[e] * 2.8853900817779268f, -60.0f, 60.0f));
            const float b = __builtin_amdgcn_exp2f(hg[e] * -1.4426950408889634f);
            z[e] = (a - 1.0f) * __builtin_amdgcn_rcpf((a + 1.0f) * (1.0f + b));
        }
#pragma unroll
        for (int h2 = 0; h2 < 2; ++h2) {
            u32x4 v = {pk(z[8 * h2], z[8 * h2 + 1]), pk(z[8 * h2 + 2], z[8 * h2 + 3]), pk(z[8 * h2 + 4], z[8 * h2 + 5]), pk(z[8 * h2 + 6], z[8 * h2 + 7])};
            *(u32x4*)(zb + (4 * wv + 2 * sg + h2) * 1024 + lane * 16) = v;
        }
        FINE(3, 3 + sg);
    }
    __syncthreads();
    FINE(3, 5);
    f32x16 o[2] = {(f32x16)(0.0f), (f32x16)(0.0f)};
#pragma unroll
    for (int kz = 0; kz < 16; ++kz) {
        const u32x4 zf = *(const u32x4*)(zb + kz * 1024 + lane * 16);
        o[0] = mfma32(wo[0][kz], zf, o[0]);
        o[1] = mfma32(wo[1][kz], zf, o[1]);
    }
    FINE(3, 6);
    const int m = 32 * k + r32;
    auto rs_o = __builtin_amdgcn_make_buffer_rsrc((void*)out, 0, 2 * T_LEN * 512, RS3);
    const unsigned ooff = m < L_SEQ ? ((s >> 1) * T_LEN + 2 * m + (s & 1)) * 512 + 128 * wv + 16 * hh : 0x7ffffff0;
#pragma unroll
    for (int et = 0; et < 2; ++et)
#pragma unroll
        for (int mm = 0; mm < 4; mm += 2) {
            unsigned a0 = pk(o[et][4 * mm], o[et][4 * mm + 1]), a1 = pk(o[et][4 * mm + 2], o[et][4 * mm + 3]);
            unsigned b0 = pk(o[et][4 * mm + 4], o[et][4 * mm + 5]), b1 = pk(o[et][4 * mm + 6], o[et][4 * mm + 7]);
            auto r0 = __builtin_amdgcn_permlane32_swap(a0, b0, false, false);
            auto r1 = __builtin_amdgcn_permlane32_swap(a1, b1, false, false);
            u32x4 v = {r0[0], r1[0], r0[1], r1[1]};
            __builtin_amdgcn_raw_buffer_store_b128(v, rs_o, ooff + et * 64, mm * 16, AUX_ST3);
        }
    FINE(3, 7);
    FINE(3, 8);
    FINE_OUT(3, 9);
    STAMP_END(2);
}

extern "C" void kernel_launch(void* const* d_in, const int* in_sizes, int n_in,
                              void* d_out, int out_size, void* d_ws, size_t ws_size,
                              hipStream_t stream) {
    (void)hipFuncSetAttribute((const void*)k1_forward, hipFuncAttributeMaxDynamicSharedMemorySize, 102400);
    (void)hipFuncSetAttribute((const void*)k2_gemm, hipFuncAttributeMaxDynamicSharedMemorySize, 131072);
    k1_forward<<<256, 256, 102400, stream>>>((const char*)d_in[0], (const char*)d_in[1], (const unsigned short*)d_in[2], (char*)d_ws);
    k2_gemm<<<256, 256, 131072, stream>>>((char*)d_ws);
    k3_inverse<<<252, 256, 0, stream>>>((const char*)d_ws, (char*)d_out);
}
```

```cpp
#include <hip/hip_runtime.h>
#include <hip/hip_bf16.h>
#include <utility>

typedef unsigned long long u64;
typedef __attribute__((ext_vector_type(2))) __bf16 bf16x2;
typedef __attribute__((ext_vector_type(4))) short s16x4;
typedef __attribute__((ext_vector_type(16))) float f32x16;
typedef __attribute__((ext_vector_type(4))) float f32x4;
typedef __attribute__((ext_vector_type(4))) unsigned u32x4;
typedef __attribute__((ext_vector_type(2))) unsigned u32x2;
typedef __attribute__((address_space(3))) void* lds_ptr;
typedef __attribute__((address_space(3))) s16x4* lds_s16x4_ptr;
typedef decltype(__builtin_amdgcn_make_buffer_rsrc((void*)0, 0, 0, 0)) rsrc_t;
#define DI __device__ __forceinline__
#define INL __attribute__((always_inline))
#define GAP() __builtin_amdgcn_sched_barrier(0)
template <class F, int... Is> DI void static_for_impl(F&& f, std::integer_sequence<int, Is...>) { (f(std::integral_constant<int, Is>{}), ...); }
template <int N, class F> DI void static_for(F&& f) { static_for_impl(f, std::make_integer_sequence<int, N>{}); }

#define PF_N 3
#define IN_POL ""
#define SLEEP 2
constexpr int SC1 = 16;
constexpr int T_LEN = 4000, L_SEQ = 2000;
constexpr unsigned XF = 262144;
constexpr unsigned WF = 524288;
constexpr unsigned YT = 65536;
constexpr unsigned X_OFF = 0;
constexpr unsigned W_OFF = 8u << 20;
constexpr unsigned Y_OFF = 24u << 20;
constexpr unsigned F_OFF = 40u << 20;
constexpr unsigned E_OFF = 41u << 20;
constexpr unsigned O_OFF = (41u << 20) + 4096;
constexpr unsigned FK = 32768;
constexpr unsigned WS_USED = 42u << 20;
constexpr unsigned RS3 = 0x00020000;
constexpr int LDS_BYTES = 147456;
constexpr int L_W0 = 36864, L_Z = 131072;

#define STAMP(slot, which)
#define STAMP_END(slot)
#define FINE_DECL
#define FINE(i) GAP()
#define FINE_OUT(n)

DI unsigned pk(float a, float b) {
    bf16x2 r = {(__bf16)a, (__bf16)b};
    return __builtin_bit_cast(unsigned, r);
}
DI float bf_lo(unsigned u) { return __builtin_bit_cast(float, u << 16); }
DI float bf_hi(unsigned u) { return __builtin_bit_cast(float, u & 0xffff0000u); }
DI u32x2 tr_read(const char* p) {
    return __builtin_bit_cast(u32x2, __builtin_amdgcn_ds_read_tr16_b64_v4i16((lds_s16x4_ptr)p));
}
DI void cos_pair(int i0, int i1, float scale, unsigned& hi, unsigned& lo) {
    float c0 = __builtin_amdgcn_cosf((float)(i0 & 127) * (1.0f / 128.0f)) * scale;
    float c1 = __builtin_amdgcn_cosf((float)(i1 & 127) * (1.0f / 128.0f)) * scale;
    hi = pk(c0, c1);
    lo = pk(c0 - bf_lo(hi), c1 - bf_hi(hi));
}

DI void m16_0(f32x4& d, u32x4 a, u32x4 b) { asm volatile("v_mfma_f32_16x16x32_bf16 %0, %1, %2, 0" : "=&v"(d) : "v"(a), "v"(b)); }
DI void m16(f32x4& d, u32x4 a, u32x4 b) { asm volatile("v_mfma_f32_16x16x32_bf16 %0, %1, %2, %0" : "+v"(d) : "v"(a), "v"(b)); }
DI void m32_0(f32x16& d, u32x4 a, u32x4 b) { asm volatile("s_nop 1\n\tv_mfma_f32_32x32x16_bf16 %0, %1, %2, 0" : "=&v"(d) : "v"(a), "v"(b)); }
DI void m32(f32x16& d, u32x4 a, u32x4 b) { asm volatile("s_nop 1\n\tv_mfma_f32_32x32x16_bf16 %0, %1, %2, %0" : "+v"(d) : "v"(a), "v"(b)); }
template <int R, bool FIRST> DI void m32_va(f32x16& d, u32x4 a) {
    if constexpr (FIRST) asm volatile("v_mfma_f32_32x32x16_bf16 %0, %1, a[%c2:%c3], 0" : "=&v"(d) : "v"(a), "i"(R), "i"(R + 3));
    else asm volatile("v_mfma_f32_32x32x16_bf16 %0, %1, a[%c2:%c3], %0" : "+v"(d) : "v"(a), "i"(R), "i"(R + 3));
}
template <int R, bool FIRST> DI void m32_av(f32x16& d, u32x4 b) {
    if constexpr (FIRST) asm volatile("s_nop 1\n\tv_mfma_f32_32x32x16_bf16 %0, a[%c2:%c3], %1, 0" : "=&v"(d) : "v"(b), "i"(R), "i"(R + 3));
    else asm volatile("s_nop 1\n\tv_mfma_f32_32x32x16_bf16 %0, a[%c2:%c3], %1, %0" : "+v"(d) : "v"(b), "i"(R), "i"(R + 3));
}
#define PIN2(a, b) asm volatile("" : "+v"(a), "+v"(b))
#define MFMA_DRAIN() do { GAP(); asm volatile("s_nop 7\n\ts_nop 7" ::: "memory"); GAP(); } while (0)

#define WAIT_VM(n) asm volatile("s_waitcnt vmcnt(" #n ")" ::: "memory")
#define BARRIER() do { __builtin_amdgcn_s_barrier(); asm volatile("" ::: "memory"); } while (0)
DI void blds16_nt(u32x4 rs, unsigned voff, unsigned soff, unsigned ldsaddr) {
    asm volatile("s_mov_b32 m0, %2\n\ts_nop 0\n\tbuffer_load_dwordx4 %0, %1, %3 offen " IN_POL " lds" :: "v"(voff), "s"(rs), "s"(ldsaddr), "s"(soff) : "memory");
}
DI void glds16(const char* sbase, unsigned voff, unsigned ldsaddr) {
    asm volatile("s_mov_b32 m0, %2\n\ts_nop 0\n\tglobal_load_lds_dwordx4 %0, %1" :: "v"(voff), "s"(sbase), "s"(ldsaddr) : "memory");
}
template <int OFF> DI void glds16_sc1(const char* sbase, unsigned voff, unsigned ldsaddr) {
    asm volatile("s_mov_b32 m0, %2\n\ts_nop 0\n\tglobal_load_lds_dwordx4 %0, %1 offset:%3 sc1" :: "v"(voff), "s"(sbase), "s"(ldsaddr), "i"(OFF) : "memory");
}
template <int R, int OFF> DI void gload_a(const char* sbase, unsigned voff) {
    asm volatile("global_load_dwordx4 a[%c2:%c3], %0, %1 offset:%4 sc1" :: "v"(voff), "s"(sbase), "i"(R), "i"(R + 3), "i"(OFF) : "memory");
}
template <int R> DI void touch_a(const char* sbase, unsigned voff) {
    asm volatile("global_load_dword a%c2, %0, %1" :: "v"(voff), "s"(sbase), "i"(R) : "memory");
}
template <int R, int OFF> DI void tr_read_a(unsigned addr) {
    asm volatile("ds_read_b64_tr_b16 a[%c1:%c2], %0 offset:%3" :: "v"(addr), "i"(R), "i"(R + 1), "i"(OFF) : "memory");
}
DI u32x4 make_rs(const void* p, unsigned bytes) {
    const u64 a = (u64)p;
    u32x4 r = {(unsigned)a, (unsigned)(a >> 32) & 0xffffu, bytes, RS3};
    return r;
}
DI void flag_set(rsrc_t rs, unsigned kind, unsigned voff, u32x2 tag) {
    __builtin_amdgcn_raw_buffer_store_b64(tag, rs, voff, kind * FK, SC1);
}
template <int R> DI void poll_issue(const char* fbase, unsigned voff) {
    asm volatile("global_load_dwordx2 a[%c2:%c3], %0, %1 sc1" :: "v"(voff), "s"(fbase), "i"(R), "i"(R + 1) : "memory");
}
template <int R> DI u32x2 poll_value() {
    u32x2 v;
    asm volatile("v_accvgpr_read_b32 %0, a%c2\n\tv_accvgpr_read_b32 %1, a%c3\n\ts_nop 0" : "=v"(v.x), "=v"(v.y) : "i"(R), "i"(R + 1) : "memory");
    return v;
}
DI void poll_spin(const char* fbase, unsigned voff, u32x2 tag, u32x2 v) {
    bool miss = v.x != tag.x || v.y != tag.y;
    for (int spins = 0; spins < (1 << 16) && __any(miss); ++spins) {
        __builtin_amdgcn_s_sleep(SLEEP);
        if (miss) {
            asm volatile("global_load_dwordx2 %0, %1, %2 sc1\n\ts_waitcnt vmcnt(0)" : "=&v"(v) : "v"(voff), "s"(fbase) : "memory");
            miss = v.x != tag.x || v.y != tag.y;
        }
    }
}
template <int R> DI void gather16_a(const char* sbase, unsigned voff) {
    asm volatile("global_load_ushort a%c2, %0, %1" :: "v"(voff), "s"(sbase), "i"(R) : "memory");
}

template <bool X>
DI void dft_task(const char* img, int ta, const u32x4 (&fh)[2], const u32x4 (&fl)[2], rsrc_t rs, unsigned voff) {
    constexpr int NB = X ? 9 : 8;
    constexpr unsigned plane = X ? XF : WF;
    u32x4 v[2][NB];
    f32x4 acc[2][8];
    auto reads = [&](int ct) INL {
        const char* t = img + (ta ^ (32 * ct));
#pragma unroll
        for (int b = 0; b < NB; ++b) {
            u32x2 v0 = tr_read(t + b * 4096), v1 = tr_read(t + b * 4096 + 512);
            v[ct & 1][b] = u32x4{v0.x, v0.y, v1.x, v1.y};
        }
    };
    auto two = [&](int ct, int j) INL {
        f32x4 (&a)[8] = acc[ct & 1];
        const u32x4 (&u)[NB] = v[ct & 1];
        m16_0(a[j], fh[0], u[j]); m16_0(a[j + 1], fh[0], u[j + 1]); m16(a[j], fl[0], u[j]); m16(a[j + 1], fl[0], u[j + 1]);
        if constexpr (X) { m16(a[j], fh[1], u[j + 1]); m16(a[j + 1], fh[1], u[j + 2]); m16(a[j], fl[1], u[j + 1]); m16(a[j + 1], fl[1], u[j + 2]); }
    };
    auto out = [&](int ct) INL {
        f32x4 (&a)[8] = acc[ct & 1];
        asm volatile("" : "+v"(a[0]), "+v"(a[1]), "+v"(a[2]), "+v"(a[3]), "+v"(a[4]), "+v"(a[5]), "+v"(a[6]), "+v"(a[7]));
        const unsigned co = X ? ct * 2048 : (ct >> 1) * 32768 + (ct & 1) * 256;
#pragma unroll
        for (int i = 0; i < 4; ++i) {
            u32x4 o = {pk(a[0][i], a[1][i]), pk(a[2][i], a[3][i]), pk(a[4][i], a[5][i]), pk(a[6][i], a[7][i])};
            __builtin_amdgcn_raw_buffer_store_b128(o, rs, voff, co + (i & 1) * 1024 + (i >> 1) * plane, SC1);
        }
    };
    reads(0);
    GAP();
#pragma unroll
    for (int ct = 0; ct < 4; ++ct) {
        two(ct, 0);
        GAP();
        if (ct < 3) reads(ct + 1);
        GAP();
        two(ct, 2);
        GAP();
        if (ct > 0) out(ct - 1);
        GAP();
        two(ct, 4); two(ct, 6);
        GAP();
    }
    MFMA_DRAIN();
    out(3);
    GAP();
}

struct XFrag { u32x2 a0, a1, b0, b1; };
constexpr int step_g(int st) { return st < 32 ? (st >> 2) & 3 : (st - 32) >> 3; }
constexpr int step_ks(int st) { return st < 32 ? 4 * (st >> 4) + (st & 3) : 8 + (st & 7); }

extern "C" __global__ __launch_bounds__(256, 1) void wavenet_fused(const char* __restrict__ x, const char* __restrict__ w,
                                                                    const char* __restrict__ wo, char* __restrict__ ws, char* __restrict__ out) {
    extern __shared__ __attribute__((aligned(16))) char lds[];
    STAMP(0, 0);
    FINE_DECL;
    FINE(0);
    asm volatile("" ::: "a0", "a127", "a128", "a255");
    const int bid = blockIdx.x, tid = threadIdx.x, lane = tid & 63;
    const int wv = __builtin_amdgcn_readfirstlane(tid >> 6);
    if (bid < 32 && wv == 3) {
        const char* pc;
        asm volatile("s_getpc_b64 %0" : "=s"(pc));
        static_for<PF_N>([&](auto I) INL { touch_a<8 + I.value>(pc, lane * 128 + I.value * 8192); });
    }
    const unsigned ldsb = (unsigned)(size_t)(lds_ptr)lds;
    const char* fbase = ws + F_OFF;
    poll_issue<4>(fbase, E_OFF - F_OFF);

    const bool heavy = (bid >> 3) & 1;
    const int tk = (bid & 7) * 16 + (bid >> 4);
    const int srow = lane >> 3, schunk = (lane & 7) ^ (srow & 2);
    const int oc = tk >> 2, cq4 = tk & 3, sq = oc >> 3, o8 = oc & 7;
    const int oct = (tk >> 3) + (heavy ? 16 : 0), dg = (tk & 7) ^ (heavy ? 1 : 0);
    if (heavy) {
        const u32x4 rs = make_rs(x, 2 * T_LEN * 512);
        const unsigned v0 = srow * 1024 + schunk * 16, v1 = v0 ^ 64;
        const int base = ((sq >> 1) * T_LEN + (sq & 1)) * 512 + cq4 * 128, m0 = 256 * o8 - 32 + 72 * wv;
#pragma unroll
        for (int i = 0; i < 9; ++i) {
            const int m = m0 + 8 * i;
            blds16_nt(rs, ((wv + i) & 1) ? v1 : v0, (unsigned)m < (unsigned)L_SEQ ? base + m * 1024 : 2 * T_LEN * 512, ldsb + (wv * 9 + i) * 1024);
        }
    }
    {
        const u32x4 rs = make_rs(w, 31 * 256 * 1024);
        const unsigned v0 = srow * 262144 + schunk * 16, v1 = v0 ^ 64;
        const unsigned base = (8 * oct + 2 * wv) * 1024 + dg * 128;
#pragma unroll
        for (int i = 0; i < 8; ++i)
            blds16_nt(rs, (i & 1) ? v1 : v0, base + (i >> 2) * 1024 + (i & 3) * 2097152, ldsb + L_W0 + (wv * 8 + i) * 1024);
    }
    const bool wo_maker = !heavy && bid < 32;
    const int kzm = (bid & 7) + (bid >> 4) * 8;
    if (wo_maker) {
        static_for<16>([&](auto I) INL {
            constexpr int j = I.value & 7, et = I.value >> 3;
            gather16_a<16 + I.value>(wo + (16 * kzm + 8 * (j >> 2) + (j & 3)) * 512 + (2 * wv + et) * 64, (lane >> 5) * 2048 + (lane & 31) * 2);
        });
    }
    FINE(1);
    const int g4 = lane >> 4, q = (lane >> 2) & 3, p = lane & 3, col = lane & 15;
    const int hh = lane >> 5, r32 = lane & 31;
    u32x4 fh[2], fl[2];
    {
        const int rho = 16 * wv + col, fr = (rho | 1), ph = 32 * (rho & 1);
#pragma unroll
        for (int kb = 0; kb < 2; ++kb)
#pragma unroll
            for (int j = 0; j < 4; ++j) {
                const int n = 32 * kb + 8 * g4 + 2 * j;
                unsigned a, b; cos_pair(fr * n + ph, fr * (n + 1) + ph, 1.0f, a, b); fh[kb][j] = a; fl[kb][j] = b;
            }
        asm volatile("" : "+v"(fh[0]), "+v"(fh[1]), "+v"(fl[0]), "+v"(fl[1]));
    }
    const rsrc_t rs_ws = __builtin_amdgcn_make_buffer_rsrc((void*)ws, 0, WS_USED, RS3);
    const int ta = (8 * g4 + q) * 128 + (((q >> 1) | ((g4 & 1) << 1)) * 32) + 8 * p;
    const int f0 = 8 * wv + 2 * g4;
    FINE(2);
    if (heavy) WAIT_VM(8); else WAIT_VM(0);
    BARRIER();
    u32x2 tag = poll_value<4>();
    tag.x = __builtin_amdgcn_readfirstlane(tag.x) + 1;
    tag.y = __builtin_amdgcn_readfirstlane(tag.y);
    const unsigned myflag = lane == 0 ? F_OFF + bid * 128 : 0x7ffffff0u;
    auto publish = [&](int kind) INL { BARRIER(); if (wv == 0) flag_set(rs_ws, kind, myflag, tag); };
    FINE(3);
    if (heavy) {
        const int og = o8 & 3;
        dft_task<true>(lds, ta, fh, fl, rs_ws, X_OFF + f0 * XF + (oc >> 2) * 32768 + cq4 * 8192 + og * 256 + ((col ^ (4 * og)) * 16));
        FINE(4);
        WAIT_VM(16); BARRIER();
    } else if (wo_maker) {
        static_for<2>([&](auto E) INL {
            constexpr int r = 16 + 8 * E.value;
            const u32x2 a = poll_value<r>(), b = poll_value<r + 2>(), c = poll_value<r + 4>(), d = poll_value<r + 6>();
            const u32x4 v = {a.x | (a.y << 16), b.x | (b.y << 16), c.x | (c.y << 16), d.x | (d.y << 16)};
            __builtin_amdgcn_raw_buffer_store_b128(v, rs_ws, O_OFF + ((2 * wv + E.value) * 16 + kzm) * 1024 + lane * 16, 0, SC1);
        });
    }
    FINE(5);
    dft_task<false>(lds + L_W0, ta, fh, fl, rs_ws, W_OFF + f0 * WF + dg * 65536 + (oct >> 1) * 2048 + (oct & 1) * 512 + col * 16);
    FINE(8);
    if (heavy) WAIT_VM(16); else WAIT_VM(0);
    publish(0);
    FINE(9);
    u32x4 gh[4], gl[4];
#pragma unroll
    for (int ks = 0; ks < 4; ++ks) {
#pragma unroll
        for (int j = 0; j < 4; ++j) {
            const int i0 = (2 * (8 * ks + 4 * hh + j) + 1) * (32 + r32);
            unsigned a, b; cos_pair(i0, i0 + 32, 1.0f / 32.0f, a, b); gh[ks][j] = a; gl[ks][j] = b;
        }
        asm volatile("" : "+v"(gh[ks]), "+v"(gl[ks]));
    }
    FINE(10);
    FINE(11);

    const int f = (bid & 7) * 4 + (bid >> 6), th = (bid >> 5) & 1, cg = ((bid >> 3) & 3) * 4 + wv;
    unsigned po1, po2;
    {
        const int tka = 16 * (4 * th + wv) + (lane & 15), tkw = 8 * (lane & 15) + (cg >> 1), tku = tkw ^ 1;
        po1 = (lane & 16) ? ((tkw >> 4) | ((tkw & 15) << 4)) * 128 : ((tka >> 4) | 8 | ((tka & 15) << 4)) * 128;
        po2 = (lane & 16) ? ((lane & 7) | ((lane & 8) << 1)) * 128 : ((tku >> 4) | 8 | ((tku & 15) << 4)) * 128 + FK;
    }
    poll_issue<0>(fbase, po1);
    if (heavy) {
        WAIT_VM(1); publish(1);
        if (wv == 0) WAIT_VM(1); else WAIT_VM(0);
    } else WAIT_VM(0);
    FINE(12);
    poll_spin(fbase, po1, tag, poll_value<0>());
    FINE(13);
    const char* wsrc = ws + W_OFF + f * WF + cg * 32768 + 4096;
    const char* xsrc = ws + X_OFF + f * XF + (4 * th + wv) * 32768;
    const unsigned l16 = lane * 16, xdst = ldsb + wv * 32768;
    auto w_issue = [&](auto I) INL { constexpr int i = I.value; gload_a<4 * i, (i & 7) * 1024 - 4096>(wsrc + (i >> 3) * 8192, l16); };
    auto x_issue = [&](auto I) INL { constexpr int i = I.value; glds16_sc1<(i & 3) * 1024>(xsrc + (i >> 2) * 4096, l16, xdst + (i >> 2) * 4096); };
    const char* osrc = ws + O_OFF + wv * 32768 + 4096;
    auto o_issue = [&](auto I) INL { constexpr int i = I.value; gload_a<128 + 4 * i, (i & 7) * 1024 - 4096>(osrc + (i >> 3) * 8192, l16); };
    static_for<8>([&](auto I) INL { w_issue(I); });
    static_for<8>([&](auto I) INL { x_issue(I); });
    static_for<8>([&](auto I) INL { w_issue(std::integral_constant<int, 8 + I.value>{}); });
    static_for<8>([&](auto I) INL { x_issue(std::integral_constant<int, 8 + I.value>{}); });
    FINE(14);
    const int og = 2 * (g4 & 1) + (p >> 1);
    const int tb = og * 256 + (((8 * (g4 >> 1) + q) ^ (4 * og)) * 16) + 8 * (p & 1);
    const char* tx[4] = {lds + tb, lds + (tb ^ 64), lds + tb + 65536, lds + (tb ^ 64) + 65536};
    const unsigned yoff = Y_OFF + (128 * th + 4 * hh) * YT + f * 2048 + (32 * cg + r32) * 4;
    auto rd = [&](XFrag& xx, int st, int part) INL {
        const int g = step_g(st), o = (g & 1) * 32768 + step_ks(st) * 2048;
        if (part == 0) xx.a0 = tr_read(tx[g & 2] + o);
        if (part == 1) xx.a1 = tr_read(tx[(g & 2) + 1] + o);
        if (part == 2) xx.b0 = tr_read(tx[g & 2] + o + 1024);
        if (part == 3) xx.b1 = tr_read(tx[(g & 2) + 1] + o + 1024);
    };
    auto y_store = [&](const f32x16& yr, const f32x16& yi, int g, int e) INL {
        __builtin_amdgcn_raw_buffer_store_b32(pk(yr[e], yi[e]), rs_ws, yoff, (32 * g + (e & 3) + 8 * (e >> 2)) * YT, SC1);
    };
    XFrag xf[3];
    f32x16 yr[4], yi[4];
    WAIT_VM(16); BARRIER();
    FINE(15);
    poll_issue<126>(fbase, po2);
#pragma unroll
    for (int part = 0; part < 4; ++part) rd(xf[0], 0, part);
#pragma unroll
    for (int part = 0; part < 4; ++part) rd(xf[1], 1, part);
    GAP();
    static_for<64>([&](auto ST) INL {
        constexpr int st = ST.value, g = step_g(st), ks = step_ks(st);
        if constexpr (st == 14) {
            WAIT_VM(14);
            poll_spin(fbase, po2, tag, poll_value<126>());
            BARRIER(); FINE(16);
        }
        if constexpr (st == 30) { WAIT_VM(22); BARRIER(); FINE(17); }
        if constexpr (st == 36) { WAIT_VM(26); FINE(18); }
        if constexpr (st == 56) { WAIT_VM(16); publish(3); FINE(19); }
        const XFrag& xx = xf[st % 3];
        XFrag& nx = xf[(st + 2) % 3];
        const u32x4 xr = {xx.a0.x, xx.a0.y, xx.a1.x, xx.a1.y}, xi = {xx.b0.x, xx.b0.y, xx.b1.x, xx.b1.y};
        m32_va<8 * ks, ks == 0>(yr[g], xr);
        GAP();
        u32x4 nxi = xi ^ 0x80008000u;
        asm volatile("" : "+v"(nxi));
        if (st < 62) rd(nx, st + 2, 0);
        GAP();
        m32_va<8 * ks + 4, ks == 0>(yi[g], xr);
        GAP();
        if (st < 62) rd(nx, st + 2, 1);
        if constexpr (st < 16) x_issue(std::integral_constant<int, 16 + st>{});
        else if constexpr (st < 23) w_issue(std::integral_constant<int, 16 + 2 * (st - 15)>{});
        else if constexpr (st < 39) o_issue(std::integral_constant<int, 2 * (st - 23)>{});
        GAP();
        m32_va<8 * ks + 4, false>(yr[g], nxi);
        GAP();
        if (st < 62) rd(nx, st + 2, 2);
        if constexpr (st == 14 || st == 15) w_issue(std::integral_constant<int, 16 + (st & 1)>{});
        else if constexpr (st >= 16 && st < 23) w_issue(std::integral_constant<int, 17 + 2 * (st - 15)>{});
        else if constexpr (st >= 23 && st < 39) o_issue(std::integral_constant<int, 2 * (st - 23) + 1>{});
        if constexpr (st >= 40 && (st & 7) == 0) PIN2(yr[g - 1], yi[g - 1]);
        if constexpr (st >= 40) y_store(yr[g - 1], yi[g - 1], g - 1, 2 * (st & 7));
        GAP();
        m32_va<8 * ks, false>(yi[g], xi);
        GAP();
        if (st < 62) rd(nx, st + 2, 3);
        if constexpr (st >= 40) y_store(yr[g - 1], yi[g - 1], g - 1, 2 * (st & 7) + 1);
        GAP();
    });
    FINE(20);
    WAIT_VM(16); publish(4);
    MFMA_DRAIN();
    PIN2(yr[3], yi[3]);
#pragma unroll
    for (int e = 0; e < 16; ++e) y_store(yr[3], yi[3], 3, e);
    FINE(21);
    WAIT_VM(16); publish(5);

    const int s3 = bid >> 6, k3 = bid & 63;
    {
        const unsigned pa = ((r32 >> 2) | ((4 * (bid >> 7) + (wv >> 1) + 2 * hh) << 3) | ((r32 & 3) << 6)) * 128 + (3 + ((bid >> 5) & 3)) * FK;
        poll_issue<0>(fbase, pa);
        WAIT_VM(1); publish(6);
        FINE(22);
        if (k3 == 63) {
            if (bid == 255) {
                poll_spin(fbase, (wv * 64 + lane) * 128, tag, u32x2{~tag.x, 0});
                BARRIER();
                flag_set(rs_ws, 0, tid == 0 ? E_OFF : 0x7ffffff0u, tag);
            }
            FINE_OUT(23);
            STAMP(0, 1);
            return;
        }
        if (wv == 0) WAIT_VM(1); else WAIT_VM(0);
        poll_spin(fbase, pa, tag, poll_value<0>());
    }
    FINE(23);
    const unsigned voff = Y_OFF + bid * YT + hh * 8192 + (64 * wv + r32) * 4;
    u32x4 yf[2][4], yg[2][4];
#pragma unroll
    for (int sg = 0; sg < 2; ++sg) {
#pragma unroll
        for (int ks = 0; ks < 4; ++ks)
#pragma unroll
            for (int j = 0; j < 4; ++j) {
                yf[sg][ks][j] = __builtin_amdgcn_raw_buffer_load_b32(rs_ws, voff + sg * 128, ks * 16384 + j * 2048, SC1);
                yg[sg][ks][j] = __builtin_amdgcn_raw_buffer_load_b32(rs_ws, voff + sg * 128 + 1024, ks * 16384 + j * 2048, SC1);
            }
        GAP();
    }
    FINE(24);
    char* zb = lds + L_Z;
#pragma unroll
    for (int sg = 0; sg < 2; ++sg) {
        f32x16 hf, hg;
        m32_0(hf, yf[sg][0], gh[0]); m32_0(hg, yg[sg][0], gh[0]);
        m32(hf, yf[sg][0], gl[0]); m32(hg, yg[sg][0], gl[0]);
#pragma unroll
        for (int ks = 1; ks < 4; ++ks) {
            m32(hf, yf[sg][ks], gh[ks]); m32(hg, yg[sg][ks], gh[ks]);
            m32(hf, yf[sg][ks], gl[ks]); m32(hg, yg[sg][ks], gl[ks]);
        }
        MFMA_DRAIN();
        PIN2(hf, hg);
        float z[16];
#pragma unroll
        for (int e = 0; e < 16; ++e) {
            const float a = __builtin_amdgcn_exp2f(__builtin_amdgcn_fmed3f(hf[e] * 2.8853900817779268f, -60.0f, 60.0f));
            const float b = __builtin_amdgcn_exp2f(hg[e] * -1.4426950408889634f);
            z[e] = (a - 1.0f) * __builtin_amdgcn_rcpf((a + 1.0f) * (1.0f + b));
        }
#pragma unroll
        for (int h2 = 0; h2 < 2; ++h2) {
            u32x4 v = {pk(z[8 * h2], z[8 * h2 + 1]), pk(z[8 * h2 + 2], z[8 * h2 + 3]), pk(z[8 * h2 + 4], z[8 * h2 + 5]), pk(z[8 * h2 + 6], z[8 * h2 + 7])};
            *(u32x4*)(zb + (4 * wv + 2 * sg + h2) * 1024 + lane * 16) = v;
        }
        GAP();
        FINE(25 + sg);
    }
    asm volatile("s_waitcnt lgkmcnt(0)" ::: "memory");
    BARRIER();
    f32x16 o0, o1;
    {
        u32x4 zf[16];
#pragma unroll
        for (int kz = 0; kz < 16; ++kz) zf[kz] = *(const u32x4*)(zb + kz * 1024 + lane * 16);
        GAP();
        static_for<16>([&](auto I) INL {
            constexpr int kz = I.value;
            m32_av<128 + 4 * kz, kz == 0>(o0, zf[kz]);
            m32_av<192 + 4 * kz, kz == 0>(o1, zf[kz]);
        });
    }
    MFMA_DRAIN();
    PIN2(o0, o1);
    FINE(27);
    const int m = 32 * k3 + r32;
    const rsrc_t rs_o = __builtin_amdgcn_make_buffer_rsrc((void*)out, 0, 2 * T_LEN * 512, RS3);
    const unsigned ooff = m < L_SEQ ? ((s3 >> 1) * T_LEN + 2 * m + (s3 & 1)) * 512 + 128 * wv + 16 * hh : 0x7ffffff0u;
#pragma unroll
    for (int et = 0; et < 2; ++et) {
        const f32x16& o = et ? o1 : o0;
#pragma unroll
        for (int mm = 0; mm < 4; mm += 2) {
            unsigned a0 = pk(o[4 * mm], o[4 * mm + 1]), a1 = pk(o[4 * mm + 2], o[4 * mm + 3]);
            unsigned b0 = pk(o[4 * mm + 4], o[4 * mm + 5]), b1 = pk(o[4 * mm + 6], o[4 * mm + 7]);
            auto r0 = __builtin_amdgcn_permlane32_swap(a0, b0, false, false);
            auto r1 = __builtin_amdgcn_permlane32_swap(a1, b1, false, false);
            u32x4 v = {r0[0], r1[0], r0[1], r1[1]};
            __builtin_amdgcn_raw_buffer_store_b128(v, rs_o, ooff + et * 64, mm * 16, 0);
        }
    }
    FINE(28);
    FINE(29);
    FINE_OUT(30);
    STAMP_END(0);
}

extern "C" void kernel_launch(void* const* d_in, const int* in_sizes, int n_in,
                              void* d_out, int out_size, void* d_ws, size_t ws_size,
                              hipStream_t stream) {
    (void)hipFuncSetAttribute((const void*)wavenet_fused, hipFuncAttributeMaxDynamicSharedMemorySize, LDS_BYTES);
    wavenet_fused<<<256, 256, LDS_BYTES, stream>>>((const char*)d_in[0], (const char*)d_in[1], (const char*)d_in[2], (char*)d_ws, (char*)d_out);
}
```

```cpp
#include <hip/hip_runtime.h>
#include <hip/hip_bf16.h>
#include <utility>

typedef unsigned long long u64;
typedef __attribute__((ext_vector_type(2))) __bf16 bf16x2;
typedef __attribute__((ext_vector_type(4))) short s16x4;
typedef __attribute__((ext_vector_type(16))) float f32x16;
typedef __attribute__((ext_vector_type(4))) float f32x4;
typedef __attribute__((ext_vector_type(8))) unsigned u32x8;
typedef __attribute__((ext_vector_type(4))) unsigned u32x4;
typedef __attribute__((ext_vector_type(2))) unsigned u32x2;
typedef __attribute__((address_space(3))) void* lds_ptr;
typedef __attribute__((address_space(3))) s16x4* lds_s16x4_ptr;
typedef decltype(__builtin_amdgcn_make_buffer_rsrc((void*)0, 0, 0, 0)) rsrc_t;
using std::integral_constant;
#define DI __device__ __forceinline__
#define INL __attribute__((always_inline))
#define GAP() __builtin_amdgcn_sched_barrier(0)
#define IC(n) integral_constant<int, (n)>{}
template <class F, int... Is> DI void static_for_impl(F&& f, std::integer_sequence<int, Is...>) { (f(integral_constant<int, Is>{}), ...); }
template <int N, class F> DI void static_for(F&& f) { static_for_impl(f, std::make_integer_sequence<int, N>{}); }

constexpr int T_LEN = 4000, L_SEQ = 2000;
constexpr unsigned X_BYTES = 2 * T_LEN * 512, W_BYTES = 31 * 256 * 1024;
constexpr unsigned XF = 262144 + 128;
constexpr unsigned WF = 524288 + 128;
constexpr unsigned YT = 65536 + 128;
constexpr unsigned X_OFF = 0, W_OFF = (8u << 20) + 65536, Y_OFF = (24u << 20) + 131072;
constexpr unsigned F_OFF = (40u << 20) + 262144;
constexpr unsigned FK = 262144;
constexpr unsigned E_OFF = (42u << 20) + 262144;
constexpr unsigned WS_USED = 43u << 20, OOB = 0x7ffffff0u, RS3 = 0x00020000;
constexpr int SC1 = 16;
constexpr int L_T2 = 36864, L_Z = 131072, L_G = 147456, LDS_BYTES = 155648;

DI unsigned pk(float a, float b) { bf16x2 r = {(__bf16)a, (__bf16)b}; return __builtin_bit_cast(unsigned, r); }
DI float cos124(int i) { return __builtin_amdgcn_cosf((float)(i - 124 * ((i * 8457) >> 20)) * (1.0f / 124.0f)); }
DI void cos_pair(int i0, int i1, float scale, unsigned& hi, unsigned& lo) {
    const float c0 = cos124(i0) * scale, c1 = cos124(i1) * scale;
    hi = pk(c0, c1);
    lo = pk(c0 - __builtin_bit_cast(float, hi << 16), c1 - __builtin_bit_cast(float, hi & 0xffff0000u));
}
DI u32x2 tr_read(const char* p) { return __builtin_bit_cast(u32x2, __builtin_amdgcn_ds_read_tr16_b64_v4i16((lds_s16x4_ptr)p)); }
DI unsigned sel(bool c, unsigned a, unsigned b) { unsigned r; asm("s_cmp_lg_u32 %1, 0\n\ts_cselect_b32 %0, %2, %3" : "=s"(r) : "s"((unsigned)c), "s"(a), "s"(b) : "scc"); return r; }
DI u32x4 make_rs(const void* p, unsigned bytes) { const u64 a = (u64)p; return u32x4{(unsigned)a, (unsigned)(a >> 32) & 0xffffu, bytes, RS3}; }

DI void m16_0(f32x4& d, u32x4 a, u32x4 b) { asm volatile("v_mfma_f32_16x16x32_bf16 %0, %1, %2, 0" : "=&v"(d) : "v"(a), "v"(b)); }
DI void m16(f32x4& d, u32x4 a, u32x4 b) { asm volatile("v_mfma_f32_16x16x32_bf16 %0, %1, %2, %0" : "+v"(d) : "v"(a), "v"(b)); }
DI void m32_0(f32x16& d, u32x4 a, u32x4 b) { asm volatile("v_mfma_f32_32x32x16_bf16 %0, %1, %2, 0" : "=&v"(d) : "v"(a), "v"(b)); }
DI void m32(f32x16& d, u32x4 a, u32x4 b) { asm volatile("v_mfma_f32_32x32x16_bf16 %0, %1, %2, %0" : "+v"(d) : "v"(a), "v"(b)); }
template <int R, bool FIRST> DI void m16_va(f32x4& d, u32x4 a) {
    if constexpr (FIRST) asm volatile("v_mfma_f32_16x16x32_bf16 %0, %1, a[%c2:%c3], 0" : "=&v"(d) : "v"(a), "i"(R), "i"(R + 3));
    else asm volatile("v_mfma_f32_16x16x32_bf16 %0, %1, a[%c2:%c3], %0" : "+v"(d) : "v"(a), "i"(R), "i"(R + 3));
}
template <int R, bool FIRST> DI void m32_av(f32x16& d, u32x4 b) {
    if constexpr (FIRST) asm volatile("v_mfma_f32_32x32x16_bf16 %0, a[%c2:%c3], %1, 0" : "=&v"(d) : "v"(b), "i"(R), "i"(R + 3));
    else asm volatile("v_mfma_f32_32x32x16_bf16 %0, a[%c2:%c3], %1, %0" : "+v"(d) : "v"(b), "i"(R), "i"(R + 3));
}
#define MFMA_DRAIN() do { GAP(); asm volatile("s_nop 7\n\ts_nop 7" ::: "memory"); GAP(); } while (0)
template <class A> DI void pin(A& a) { asm volatile("" : "+v"(a)); }
template <class A, class B> DI void pin(A& a, B& b) { asm volatile("" : "+v"(a), "+v"(b)); }

template <int N> DI void wait_vm() { asm volatile("s_waitcnt vmcnt(%0)" :: "i"(N) : "memory"); }
DI void wait_lds() { asm volatile("s_waitcnt lgkmcnt(0)" ::: "memory"); }
DI void barrier() { __builtin_amdgcn_s_barrier(); asm volatile("" ::: "memory"); }
DI void blds16(u32x4 rs, unsigned voff, unsigned soff, unsigned ldsaddr) {
    asm volatile("s_mov_b32 m0, %2\n\ts_nop 0\n\tbuffer_load_dwordx4 %0, %1, %3 offen lds" :: "v"(voff), "s"(rs), "s"(ldsaddr), "s"(soff) : "memory");
}
template <int OFF> DI void glds16_sc1(const char* sbase, unsigned voff, unsigned ldsaddr) {
    if constexpr (OFF == 0) asm volatile("s_mov_b32 m0, %2\n\ts_nop 0\n\tglobal_load_lds_dwordx4 %0, %1 sc1" :: "v"(voff), "s"(sbase), "s"(ldsaddr) : "memory");
    else asm volatile("global_load_lds_dwordx4 %0, %1 offset:%2 sc1" :: "v"(voff), "s"(sbase), "i"(OFF) : "memory");
}
template <int R, int OFF> DI void gload_a(const char* sbase, unsigned voff) {
    asm volatile("global_load_dwordx4 a[%c2:%c3], %0, %1 offset:%4 sc1" :: "v"(voff), "s"(sbase), "i"(R), "i"(R + 3), "i"(OFF) : "memory");
}
template <int R> DI void touch_a(const char* sbase, unsigned voff) {
    asm volatile("global_load_dword a%c2, %0, %1" :: "v"(voff), "s"(sbase), "i"(R) : "memory");
}
template <int R, int OFF> DI void tr_read_a(unsigned addr) {
    asm volatile("ds_read_b64_tr_b16 a[%c1:%c2], %0 offset:%3" :: "v"(addr), "i"(R), "i"(R + 1), "i"(OFF) : "memory");
}

DI void flag_set(rsrc_t rs, unsigned kind, unsigned voff, u32x2 tag) {
    __builtin_amdgcn_raw_buffer_store_b128(u32x4{tag.x, tag.y, tag.x, tag.y}, rs, voff, kind * FK, SC1);
}
template <int R> DI void poll_issue(const char* base, unsigned voff) {
    asm volatile("global_load_dwordx2 a[%c2:%c3], %0, %1 sc1" :: "v"(voff), "s"(base), "i"(R), "i"(R + 1) : "memory");
}
template <int R> DI u32x2 poll_value() {
    u32x2 v;
    asm volatile("v_accvgpr_read_b32 %0, a%c2\n\tv_accvgpr_read_b32 %1, a%c3\n\ts_nop 0" : "=v"(v.x), "=v"(v.y) : "i"(R), "i"(R + 1) : "memory");
    return v;
}
DI void poll_spin(const char* base, unsigned voff, u32x2 tag, u32x2 v) {
    const u64 t = __builtin_bit_cast(u64, tag);
    bool miss = __builtin_bit_cast(u64, v) != t;
    if (__builtin_expect(__builtin_amdgcn_ballot_w64(miss) != 0, 0)) {
        for (int spins = 0; spins < (1 << 16); ++spins) {
            __builtin_amdgcn_s_sleep(2);
            if (miss) {
                asm volatile("global_load_dwordx2 %0, %1, %2 sc1\n\ts_waitcnt vmcnt(0)" : "=&v"(v) : "v"(voff), "s"(base) : "memory");
                miss = __builtin_bit_cast(u64, v) != t;
            }
            if (__builtin_amdgcn_ballot_w64(miss) == 0) break;
        }
    }
}

template <bool X, int NCT, class H>
DI void dft_task(const char* img, int ta, const u32x4 (&fh)[2], const u32x4 (&fl)[2], rsrc_t rs, unsigned voff, H&& before_stores) {
    constexpr int NB = X ? 9 : 8;
    constexpr unsigned plane = X ? XF : WF;
    const unsigned voff1 = threadIdx.x >= 240 ? OOB : voff;
    u32x4 v[2][NB];
    f32x4 acc[2][8];
    auto reads = [&](int ct) INL {
        const char* t = img + (ta ^ (32 * ct));
#pragma unroll
        for (int b = 0; b < NB; ++b) {
            const u32x2 v0 = tr_read(t + b * 4096), v1 = tr_read(t + b * 4096 + 512);
            v[ct & 1][b] = u32x4{v0.x, v0.y, v1.x, v1.y};
        }
    };
    auto two = [&](int ct, int j) INL {
        f32x4 (&a)[8] = acc[ct & 1];
        const u32x4 (&u)[NB] = v[ct & 1];
        m16_0(a[j], fh[0], u[j]); m16_0(a[j + 1], fh[0], u[j + 1]); m16(a[j], fl[0], u[j]); m16(a[j + 1], fl[0], u[j + 1]);
        if constexpr (X) { m16(a[j], fh[1], u[j + 1]); m16(a[j + 1], fh[1], u[j + 2]); m16(a[j], fl[1], u[j + 1]); m16(a[j + 1], fl[1], u[j + 2]); }
    };
    auto out = [&](int ct) INL {
        f32x4 (&a)[8] = acc[ct & 1];
        asm volatile("" : "+v"(a[0]), "+v"(a[1]), "+v"(a[2]), "+v"(a[3]), "+v"(a[4]), "+v"(a[5]), "+v"(a[6]), "+v"(a[7]));
        const unsigned co = X ? (ct >> 1) * 4096 + (ct & 1) * 256 : ct * 32768;
#pragma unroll
        for (int i = 0; i < 4; ++i) {
            const u32x4 o = {pk(a[0][i], a[1][i]), pk(a[2][i], a[3][i]), pk(a[4][i], a[5][i]), pk(a[6][i], a[7][i])};
            __builtin_amdgcn_raw_buffer_store_b128(o, rs, (i >> 1) ? voff1 : voff, co + (i & 1) * 2048 + (i >> 1) * plane, SC1);
        }
    };
    reads(0);
    GAP();
#pragma unroll
    for (int ct = 0; ct < NCT; ++ct) {
        two(ct, 0);
        GAP();
        if (ct < NCT - 1) reads(ct + 1);
        GAP();
        two(ct, 2); two(ct, 4);
        GAP();
        if (NCT > 2 && ct == 1) before_stores();
        if (NCT > 2 && ct > 0) out(ct - 1);
        GAP();
        two(ct, 6);
        GAP();
    }
    MFMA_DRAIN();
    if (NCT == 2) { before_stores(); out(0); }
    out(NCT - 1);
    GAP();
}

struct XFrag { u32x2 a0, a1, b0, b1; };
constexpr int step_g(int st) { return st < 16 ? (st >> 1) & 3 : st < 32 ? (st >> 2) & 3 : (st - 32) >> 3; }
constexpr int step_ks(int st) { return st < 16 ? 2 * (st >> 3) + (st & 1) : st < 32 ? 4 + (st & 3) : 8 + (st & 7); }

extern "C" __global__ __launch_bounds__(256, 1) void wavenet_fused(const char*, const char*, const char*, char*, char*) {
    u32x8 ka; u64 kb;
    asm volatile("s_load_dwordx8 %0, %2, 0x0\n\ts_load_dwordx2 %1, %2, 0x20" : "=&s"(ka), "=&s"(kb) : "s"(__builtin_amdgcn_kernarg_segment_ptr()) : "memory");
    extern __shared__ __attribute__((aligned(16))) char lds[];
    asm volatile("" ::: "a0", "a127", "a128", "a255");
    const int bid = blockIdx.x, tid = threadIdx.x, lane = tid & 63;
    const int wv = __builtin_amdgcn_readfirstlane(tid >> 6);
    {
        u64 pc; unsigned t0, t1;
        asm volatile("s_cmp_eq_u32 %4, 3\n\ts_cselect_b32 %1, %3, 32\n\ts_cmp_lt_u32 %1, 32\n\ts_cbranch_scc0 1f\n\t"
                     "s_getpc_b64 %0\n\t"
                     "global_load_dword a8, %5, %0\n\tv_add_u32 %2, 0x2000, %5\n\tglobal_load_dword a9, %2, %0\n\t"
                     "v_add_u32 %2, 0x4000, %5\n\tglobal_load_dword a10, %2, %0\n\t"
                     "v_add_u32 %2, 0x6000, %5\n\tv_min_u32 %2, 0x6500, %2\n\tglobal_load_dword a11, %2, %0\n"
                     "1:" : "=&s"(pc), "=&s"(t0), "=&v"(t1) : "s"(bid), "s"(wv), "v"(lane * 128) : "memory", "scc");
    }
    GAP();
    const unsigned ldsb = (unsigned)(size_t)(lds_ptr)lds;

    const bool xt = (bid >> 3) & 1;
    const int tk = (bid & 7) * 16 + (bid >> 4);
    const int srow = lane >> 3, schunk = (lane & 7) ^ (srow & 2);
    const int oc = tk >> 2, cq4 = tk & 3, sq = oc >> 3, o8 = oc & 7;
    const int oct = tk >> 3, dg = tk & 7, dg2 = dg ^ 1;
    const unsigned t_base = sel(xt, ((sq >> 1) * T_LEN + (sq & 1)) * 512 + cq4 * 128, 0);
    const unsigned t_r0 = sel(xt, (256 * o8 - 32 + 72 * wv) * 1024, oct * 8192 + dg * 128 + 2048 * wv);
    const unsigned t_sa = sel(xt, 8192, 2097152), t_sb = sel(xt, 32768, 1024), t_lim = sel(xt, L_SEQ * 1024, 0xffffffffu);
    const unsigned t_v0 = (srow << sel(xt, 10, 18)) + (schunk ^ ((wv & (bid >> 3) & 1) << 2)) * 16;
    const int t_n = 8 + xt;
    GAP();
    asm volatile("s_waitcnt lgkmcnt(0)" : "+s"(ka), "+s"(kb) :: "memory");
    const char* w = (const char*)(((u64)ka[3] << 32) | ka[2]);
    const char* wo = (const char*)(((u64)ka[5] << 32) | ka[4]);
    char* ws = (char*)(((u64)ka[7] << 32) | ka[6]);
    char* out = (char*)kb;
    const char* fbase = ws + F_OFF + (bid & 7) * 32768;
    poll_issue<4>(ws, E_OFF);
    {
        const u32x4 rs = {sel(xt, ka[0], ka[2]), sel(xt, ka[1], ka[3]) & 0xffffu, sel(xt, X_BYTES, W_BYTES), RS3};
#pragma unroll 1
        for (int i = 0; i < t_n; ++i) {
            const unsigned r = t_r0 + (i & 3) * t_sa + (i >> 2) * t_sb;
            blds16(rs, t_v0 ^ ((i & 1) << 6), r < t_lim ? t_base + r : X_BYTES, ldsb + (wv * t_n + i) * 1024);
        }
    }
    if (wv == 2) touch_a<12>(wo, ((bid >> 3) * 32 + (lane & 31)) * 128);
    GAP();
    const int g4 = lane >> 4, q = (lane >> 2) & 3, p = lane & 3, col = lane & 15;
    const int hh = lane >> 5, r32 = lane & 31;
    const rsrc_t rs_ws = __builtin_amdgcn_make_buffer_rsrc((void*)ws, 0, WS_USED, RS3);
    u32x4 fh[2], fl[2];
    {
        const int rho = 16 * wv + col, fr = (rho | 1), ph = 31 * (rho & 1);
#pragma unroll
        for (int kb2 = 0; kb2 < 2; ++kb2)
#pragma unroll
            for (int j = 0; j < 4; ++j) {
                const int n = 32 * kb2 + 8 * g4 + 2 * j;
                unsigned a, b; cos_pair(fr * n + ph, fr * (n + 1) + ph, 1.0f, a, b); fh[kb2][j] = a; fl[kb2][j] = b;
            }
        asm volatile("" : "+v"(fh[0]), "+v"(fh[1]), "+v"(fl[0]), "+v"(fl[1]));
    }
    GAP();
    if (bid < 8 && wv == 0) {
        wait_vm<8>();
        u32x2 t = poll_value<4>();
        t.x += 1;
        flag_set(rs_ws, 2, F_OFF + srow * 32768 + bid * 128 + (lane & 7) * 16, t);
    }
    {
        const u32x4 rs = make_rs(w, W_BYTES);
        const unsigned v0 = srow * 262144 + schunk * 16, v1 = v0 ^ 64, base = 131072 + oct * 8192 + dg2 * 128 + 2048 * wv;
#pragma unroll
        for (int i = 0; i < 8; ++i) blds16(rs, (i & 1) ? v1 : v0, base + (i >> 2) * 1024 + (i & 3) * 2097152, ldsb + L_T2 + (wv * 8 + i) * 1024);
    }
    {
        u32x4 a, b;
#pragma unroll
        for (int j = 0; j < 4; ++j) {
            const int i0 = (2 * (8 * wv + 4 * hh + j) + 1) * (32 + r32);
            unsigned u, v; cos_pair(i0, i0 + 31, (j == 3 && tid >= 224) ? 0.0f : 1.4426950408889634f / 31.0f, u, v); a[j] = u; b[j] = v;
        }
        *(u32x4*)(lds + L_G + wv * 2048 + lane * 16) = a;
        *(u32x4*)(lds + L_G + wv * 2048 + 1024 + lane * 16) = b;
        wait_lds();
    }
    const int ta = (8 * g4 + q) * 128 + (((q >> 1) | ((g4 & 1) << 1)) * 32) + 8 * p;
    const int f0 = 8 * wv + 2 * g4;
    GAP();
    wait_vm<8>(); barrier();
    const unsigned po0 = 2 * FK + (lane & 7) * 128;
    poll_issue<2>(fbase, po0);
    u32x2 tag = poll_value<4>();
    tag.x = __builtin_amdgcn_readfirstlane(tag.x) + 1;
    tag.y = __builtin_amdgcn_readfirstlane(tag.y);
    const u32x2 notag = {~tag.x, tag.y};
    const unsigned myflag = lane < 16 ? F_OFF + (2 * wv + srow) * 32768 + bid * 128 + (lane & 7) * 16 : OOB;
    auto publish = [&](int kind) INL { barrier(); flag_set(rs_ws, kind, myflag, tag); };
    auto all_started = [&]() INL { wait_vm<0>(); poll_spin(fbase, po0, tag, poll_value<2>()); };
    GAP();
    const unsigned wvo = W_OFF + f0 * WF + (oct >> 2) * 4096 + (oct & 3) * 256 + col * 16;
    if (xt) {
        const int og = o8 & 1;
        u32x4 xh[2] = {fh[0], fh[1]}, xl[2] = {fl[0], fl[1]};
        xh[0].x = g4 ? xh[0].x : 0; xl[0].x = g4 ? xl[0].x : 0;
        asm volatile("s_nop 1" : "+v"(xh[0]), "+v"(xl[0]));
        dft_task<true, 4>(lds, ta, xh, xl, rs_ws, X_OFF + f0 * XF + (oc >> 2) * 32768 + cq4 * 8192 + (o8 & 3) * 512 + ((col ^ (4 * og)) * 16), all_started);
    } else dft_task<false, 4>(lds, ta, fh, fl, rs_ws, wvo + (dg & 3) * 131072 + (dg >> 2) * 1024, all_started);
    GAP();
    barrier();
    GAP();
    dft_task<false, 2>(lds + L_T2, ta ^ (xt ? 64 : 0), fh, fl, rs_ws, wvo + 16384 + (dg2 & 3) * 131072 + (dg2 >> 2) * 1024 + (xt ? 65536 : 0),
                       [&]() INL { wait_vm<0>(); publish(0); GAP(); });
    const unsigned l16 = lane * 16;
    const u32x4 rs_o = make_rs(wo, 131072);
    const unsigned ovo = srow * 512 + wv * 128 + ((lane & 7) ^ (srow & 4)) * 16;
    const unsigned ost0 = ldsb + (wv ? wv * 32768 + 16384 : L_Z), ost1 = ldsb + wv * 32768;
    auto o_issue = [&](auto I) INL { constexpr int i = I.value; blds16(rs_o, ovo, i * 4096, (i < 16 ? ost0 : ost1) + (i & 15) * 1024); };
    const unsigned ora = ((q & 1) + 4 * (q >> 1) + 2 * hh) * 128 + ((32 * (g4 & 1) + 8 * p) ^ (64 * (q >> 1)));
    auto o_read = [&](auto HALF) INL {
        const unsigned a0 = (HALF.value ? ost1 : ost0) + ora, a1 = a0 ^ 64;
        static_for<16>([&](auto I) INL {
            constexpr int k8 = I.value >> 1, a = I.value & 1;
            tr_read_a<128 + 64 * HALF.value + 8 * k8 + 2 * a, k8 * 2048 + a * 1024>(a0);
            tr_read_a<128 + 64 * HALF.value + 8 * k8 + 4 + 2 * a, k8 * 2048 + a * 1024>(a1);
        });
        wait_lds();
    };
    static_for<16>(o_issue);
    GAP();

    const int f = (bid & 7) * 4 + (bid >> 6), th = (bid >> 5) & 1, cg = ((bid >> 3) & 3) * 4 + wv;
    unsigned po1, po2;
    {
        const int tka = 16 * (4 * th + wv) + (lane & 15), tkw = 8 * (lane & 15) + (cg >> 2) + ((lane & 16) >> 2), tku = tkw ^ 1;
        po1 = (lane & 32) ? ((tka >> 4) | 8 | ((tka & 15) << 4)) * 128 : ((tkw >> 4) | ((tkw & 15) << 4)) * 128;
        po2 = ((tku >> 4) | ((wv & 2) << 2) | ((tku & 15) << 4)) * 128 + FK;
    }
    const int s3 = bid >> 6, k3 = bid & 63, rf = r32 < 31 ? r32 : 30;
    const unsigned pa = ((rf >> 2) | ((4 * (bid >> 7) + wv) << 3) | ((rf & 3) << 6)) * 128 + (3 + ((bid >> 5) & 3)) * FK;
    const bool early = !(bid & 64);
    poll_issue<0>(fbase, po1);
    wait_vm<17>(); publish(1);
    wait_vm<1>();
    GAP();
    if (f == 31) {
        o_read(IC(0));
        static_for<16>([&](auto I) INL { o_issue(IC(16 + I.value)); });
        wait_vm<0>();
        o_read(IC(1));
        __builtin_amdgcn_s_sleep(127);
        poll_spin(fbase, 4 * FK + (bid - 64) * 128, tag, notag);
        __builtin_amdgcn_s_sleep(30);
    } else {
        poll_spin(fbase, po1, tag, poll_value<0>());
        GAP();
        const char* wsrc = ws + W_OFF + f * WF + cg * 32768 + 4096;
        const char* xsrc = ws + X_OFF + f * XF + (4 * th + wv) * 32768;
        const char* usrc = ws + X_OFF + f * XF + 4 * th * 32768 + 16384 + wv * 4096;
        auto w_issue = [&](auto I) INL { constexpr int i = I.value; gload_a<4 * i, (i & 7) * 1024 - 4096>(wsrc + (i >> 3) * 8192, l16); };
        auto x_issue = [&](auto I) INL { constexpr int i = I.value; glds16_sc1<(i & 3) * 1024>(xsrc + (i >> 2) * 4096, l16, ost1 + (i >> 2) * 4096); };
        auto u_issue = [&](auto I) INL { constexpr int i = I.value; glds16_sc1<(i & 3) * 1024>(usrc + (i >> 2) * 32768, l16, ldsb + 16384 + wv * 4096 + (i >> 2) * 32768); };
        static_for<4>([&](auto I) INL { w_issue(I); });
        static_for<4>([&](auto I) INL { x_issue(I); });
        static_for<4>([&](auto I) INL { w_issue(IC(4 + I.value)); });
        static_for<4>([&](auto I) INL { x_issue(IC(4 + I.value)); });
        o_read(IC(0));
        GAP();
        const int og = p >> 1;
        const int tb = og * 512 + (((8 * g4 + q) ^ (4 * og)) * 16) + 8 * (p & 1);
        int tb2 = tb + 65536, tb3 = (tb ^ 64) + 65536;
        pin(tb2, tb3);
        const char* tx[4] = {lds + tb, lds + (tb ^ 64), lds + tb2, lds + tb3};
        const unsigned yoff = Y_OFF + (128 * th + 4 * g4) * YT + f * 2048 + cg * 128 + col * 8;
        auto rd = [&](XFrag& xx, int st, int part) INL {
            const int g = step_g(st), o = (g & 1) * 32768 + (step_ks(st) >> 1) * 4096 + (step_ks(st) & 1) * 1024;
            if (part == 0) xx.a0 = tr_read(tx[g & 2] + o);
            if (part == 1) xx.a1 = tr_read(tx[(g & 2) + 1] + o);
            if (part == 2) xx.b0 = tr_read(tx[g & 2] + o + 2048);
            if (part == 3) xx.b1 = tr_read(tx[(g & 2) + 1] + o + 2048);
        };
        f32x4 p1[4][2][2], p2[4][2][2], p3[4][2][2];
        u32x2 yv;
        auto y_pack = [&](int g, int e, int ct) INL {
            unsigned v = pk(p1[g][e >> 2][ct][e & 3] - p2[g][e >> 2][ct][e & 3], p3[g][e >> 2][ct][e & 3]);
            pin(v);
            yv[ct] = v;
        };
        auto y_store = [&](int g, int e) INL { __builtin_amdgcn_raw_buffer_store_b64(yv, rs_ws, yoff, (32 * g + 16 * (e >> 2) + (e & 3)) * YT, SC1); };
        auto y_pin = [&](int g, int rt) INL { pin(p1[g][rt][0], p1[g][rt][1]); pin(p2[g][rt][0], p2[g][rt][1]); pin(p3[g][rt][0], p3[g][rt][1]); };
        XFrag xf[3];
        wait_vm<8>(); barrier();
        GAP();
#pragma unroll
        for (int part = 0; part < 4; ++part) rd(xf[0], 0, part);
#pragma unroll
        for (int part = 0; part < 4; ++part) rd(xf[1], 1, part);
        GAP();
        static_for<64>([&](auto ST) INL {
            constexpr int st = ST.value, g = step_g(st), ks = step_ks(st);
            if constexpr (st == 6) { wait_vm<12>(); barrier(); }
            if constexpr (st == 8) poll_issue<126>(fbase, po2);
            if constexpr (st == 12) { wait_vm<8>(); poll_spin(fbase, po2, tag, poll_value<126>()); }
            if constexpr (st == 14) { barrier(); GAP(); }
            if constexpr (st == 30) { wait_vm<20>(); wait_lds(); barrier(); GAP(); }
            if constexpr (st == 36) { wait_vm<20>(); GAP(); }
            if constexpr (st == 38) { wait_vm<12>(); barrier(); }
            if constexpr (st == 54 || st == 62) { wait_vm<5>(); publish(st == 54 ? 3 : 4); GAP(); }
            auto vm = [&](auto H) INL {
                constexpr int h = H.value;
                if constexpr (st < 4) w_issue(IC(8 + 2 * st + h));
                else if constexpr (st < 8) x_issue(IC(2 * st + h));
                else if constexpr (st < 10) u_issue(IC(2 * (st - 8) + h));
                else if constexpr (st < 12) flag_set(rs_ws, 3 + 2 * (st - 10) + h, myflag, notag);
                else if constexpr (st < 20) w_issue(IC(16 + 2 * (st - 12) + h));
                else if constexpr (st < 26) u_issue(IC(4 + 2 * (st - 20) + h));
                else if constexpr (st >= 32 && st < 40) o_issue(IC(16 + 2 * (st - 32) + h));
            };
            constexpr bool ep = st >= 40, F = ks < 2;
            constexpr int e0 = st & 7, rt = ks & 1, R = 16 * (ks >> 1);
            const XFrag& xx = xf[st % 3];
            XFrag& nx = xf[(st + 2) % 3];
            const u32x4 xr = {xx.a0.x, xx.a0.y, xx.a1.x, xx.a1.y}, xi = {xx.b0.x, xx.b0.y, xx.b1.x, xx.b1.y};
            m16_va<R, F>(p1[g][rt][0], xr);
            GAP();
            if (st < 62) rd(nx, st + 2, 0);
            GAP();
            m16_va<R, F>(p3[g][rt][0], xi);
            GAP();
            if constexpr (ep && e0 > 0) y_store(g - 1, e0 - 1);
            if constexpr (st > 40 && e0 == 0) y_store(g - 2, 7);
            GAP();
            m16_va<R + 4, F>(p3[g][rt][1], xi);
            GAP();
            if (st < 62) rd(nx, st + 2, 1);
            GAP();
            m16_va<R + 4, F>(p1[g][rt][1], xr);
            GAP();
            vm(IC(0));
            if constexpr (ep && (e0 & 3) == 0) y_pin(g - 1, e0 >> 2);
            if constexpr (ep) y_pack(g - 1, e0, 0);
            GAP();
            m16_va<R + 8, false>(p3[g][rt][0], xr);
            GAP();
            if (st < 62) rd(nx, st + 2, 2);
            GAP();
            m16_va<R + 8, F>(p2[g][rt][0], xi);
            GAP();
            if constexpr (ep) y_pack(g - 1, e0, 1);
            GAP();
            m16_va<R + 12, F>(p2[g][rt][1], xi);
            GAP();
            if (st < 62) rd(nx, st + 2, 3);
            GAP();
            m16_va<R + 12, false>(p3[g][rt][1], xr);
            GAP();
            vm(IC(1));
            GAP();
        });
        y_store(2, 7);
        MFMA_DRAIN();
        y_pin(3, 0); y_pin(3, 1);
#pragma unroll
        for (int e = 0; e < 8; ++e) { y_pack(3, e, 0); y_pack(3, e, 1); y_store(3, e); }
        GAP();
        o_read(IC(1));
        wait_vm<8>(); publish(5);
        if (early) { poll_issue<0>(fbase, pa); wait_vm<2>(); } else wait_vm<1>();
        publish(6);
    }
    GAP();

    if (k3 == 63) {
        if (bid == 255) {
            poll_spin(fbase, (wv * 64 + lane) * 128, tag, notag);
            barrier();
            flag_set(rs_ws, 0, tid < 8 ? E_OFF + tid * 16 : OOB, tag);
        }
        return;
    }
    if (early) wait_vm<1>();
    else {
        if (bid & 32) __builtin_amdgcn_s_sleep(26); else __builtin_amdgcn_s_sleep(14);
        poll_issue<0>(fbase, pa); wait_vm<0>();
    }
    poll_spin(fbase, pa, tag, poll_value<0>());
    GAP();
    const unsigned voff = Y_OFF + bid * YT + hh * 8192 + 512 * wv + r32 * 4;
    const unsigned voff1 = hh ? OOB - 1024 : voff;
    u32x4 y[4][4], gh[4], gl[4];
    auto y_load = [&](int n) INL {
        y[n >> 4][(n >> 2) & 3][n & 3] = __builtin_amdgcn_raw_buffer_load_b32(rs_ws, ((n & 15) == 15 ? voff1 : voff) + (n >> 4) * 128, ((n >> 2) & 3) * 16384 + (n & 3) * 2048, SC1);
    };
    auto y_more = [&](int slot) INL { if (slot < 16) { y_load(32 + 2 * slot); y_load(33 + 2 * slot); } };
#pragma unroll
    for (int ks = 0; ks < 4; ++ks) {
        gh[ks] = *(const u32x4*)(lds + L_G + ks * 2048 + lane * 16);
        gl[ks] = *(const u32x4*)(lds + L_G + ks * 2048 + 1024 + lane * 16);
    }
    GAP();
#pragma unroll
    for (int n = 0; n < 32; ++n) { y_load(n); if ((n & 15) == 15) GAP(); }
    char* zb = lds + L_Z;
    f32x16 h[2], o0, o1;
    float z[8], gc[2], gb[2];
    u32x4 zf[12];
    auto inv = [&](int sg, int i) INL {
        if (i == 0) m32_0(h[sg & 1], y[sg][0], gh[0]); else m32(h[sg & 1], y[sg][i >> 1], (i & 1) ? gl[i >> 1] : gh[i >> 1]);
    };
    auto gate = [&](int sg, int i) INL {
        const int e = i & ~1;
        const f32x16& hs = h[sg & 1];
        if (!(i & 1)) {
#pragma unroll
            for (int k = 0; k < 2; ++k) {
                gc[k] = __builtin_amdgcn_exp2f(__builtin_amdgcn_fmed3f(hs[2 * (e + k)], -30.0f, 30.0f));
                gb[k] = __builtin_amdgcn_exp2f(-hs[2 * (e + k) + 1]);
            }
            pin(gc[0], gc[1]); pin(gb[0], gb[1]);
        } else {
#pragma unroll
            for (int k = 0; k < 2; ++k) {
                const float t = __builtin_fmaf(gc[k], gc[k], 1.0f), u = __builtin_fmaf(gc[k], gc[k], -1.0f);
                z[e + k] = u * __builtin_amdgcn_rcpf(__builtin_fmaf(t, gb[k], t));
            }
            pin(z[e], z[e + 1]);
        }
    };
    auto z_put = [&](int sg) INL {
        *(u32x4*)(zb + (4 * wv + sg) * 1024 + lane * 16) = u32x4{pk(z[0], z[1]), pk(z[2], z[3]), pk(z[4], z[5]), pk(z[6], z[7])};
        GAP();
    };
    auto z_get = [&](int sg, int slot) INL {
#pragma unroll
        for (int i = 0; i < 4; ++i) zf[slot + i] = *(const u32x4*)(zb + (4 * i + sg) * 1024 + lane * 16);
        GAP();
    };
    auto proj = [&](auto N) INL {
        constexpr int sg = N.value >> 3, i = N.value & 7, kz = 4 * (i >> 1) + sg, slot = 4 * (sg % 3) + (i >> 1);
        if constexpr (i & 1) m32_av<132 + 8 * kz, kz == 0>(o1, zf[slot]); else m32_av<128 + 8 * kz, kz == 0>(o0, zf[slot]);
    };
#pragma unroll
    for (int i = 0; i < 8; ++i) { inv(0, i); GAP(); y_more(i); GAP(); }
#pragma unroll
    for (int sg = 1; sg < 4; ++sg) {
        inv(sg, 0);
        if (sg == 1) asm volatile("s_nop 7\n\ts_nop 2" : "+v"(h[0])); else pin(h[(sg - 1) & 1]);
        GAP();
#pragma unroll
        for (int i = 0; i < 8; ++i) { gate(sg - 1, i); y_more(8 * sg + i); GAP(); if (i < 7) inv(sg, i + 1); GAP(); }
        z_put(sg - 1);
    }
    wait_lds(); barrier();
    z_get(0, 0); z_get(1, 4); z_get(2, 8);
    pin(h[1]);
    GAP();
    static_for<8>([&](auto I) INL {
        gate(3, I.value);
        GAP();
        proj(IC(3 * I.value)); proj(IC(3 * I.value + 1));
        if constexpr (I.value < 7) proj(IC(3 * I.value + 2));
        GAP();
    });
    z_put(3);
    proj(IC(23));
    wait_lds(); barrier();
    z_get(3, 0);
    static_for<8>([&](auto I) INL { proj(IC(24 + I.value)); });
    MFMA_DRAIN();
    pin(o0, o1);
    GAP();
    {
        char* ob = lds + wv * 4096;
#pragma unroll
        for (int et = 0; et < 2; ++et) {
            const f32x16& o = et ? o1 : o0;
#pragma unroll
            for (int mm = 0; mm < 4; ++mm)
                *(u32x2*)(ob + r32 * 128 + (((4 * et + mm) ^ (r32 & 7)) * 16) + 8 * hh) = u32x2{pk(o[4 * mm], o[4 * mm + 1]), pk(o[4 * mm + 2], o[4 * mm + 3])};
        }
        const rsrc_t rs_out = __builtin_amdgcn_make_buffer_rsrc((void*)out, 0, X_BYTES, RS3);
        const unsigned obase = ((s3 >> 1) * T_LEN + (s3 & 1)) * 512 + 128 * wv + (lane & 7) * 16;
#pragma unroll
        for (int i = 0; i < 4; ++i) {
            const int row = 8 * i + srow, m = 32 * k3 + row;
            const u32x4 v = *(const u32x4*)(ob + row * 128 + (((lane & 7) ^ srow) * 16));
            __builtin_amdgcn_raw_buffer_store_b128(v, rs_out, m < L_SEQ ? obase + m * 1024 : OOB, 0, SC1);
        }
    }
}

extern "C" void kernel_launch(void* const* d_in, const int* in_sizes, int n_in,
                              void* d_out, int out_size, void* d_ws, size_t ws_size,
                              hipStream_t stream) {
    (void)hipFuncSetAttribute((const void*)wavenet_fused, hipFuncAttributeMaxDynamicSharedMemorySize, LDS_BYTES);
    wavenet_fused<<<256, 256, LDS_BYTES, stream>>>((const char*)d_in[0], (const char*)d_in[1], (const char*)d_in[2], (char*)d_ws, (char*)d_out);
}
```

```cpp
#include <hip/hip_runtime.h>
#include <hip/hip_bf16.h>
#include <utility>

typedef unsigned long long u64;
typedef __attribute__((ext_vector_type(2))) __bf16 bf16x2;
typedef __attribute__((ext_vector_type(4))) short s16x4;
typedef __attribute__((ext_vector_type(16))) float f32x16;
typedef __attribute__((ext_vector_type(4))) float f32x4;
typedef __attribute__((ext_vector_type(8))) unsigned u32x8;
typedef __attribute__((ext_vector_type(4))) unsigned u32x4;
typedef __attribute__((ext_vector_type(2))) unsigned u32x2;
typedef __attribute__((address_space(3))) void* lds_ptr;
typedef __attribute__((address_space(3))) s16x4* lds_s16x4_ptr;
typedef decltype(__builtin_amdgcn_make_buffer_rsrc((void*)0, 0, 0, 0)) rsrc_t;
using std::integral_constant;
#define DI __device__ __forceinline__
#define INL __attribute__((always_inline))
#define GAP() __builtin_amdgcn_sched_barrier(0)
#define IC(n) integral_constant<int, (n)>{}
template <class F, int... Is> DI void static_for_impl(F&& f, std::integer_sequence<int, Is...>) { (f(integral_constant<int, Is>{}), ...); }
template <int N, class F> DI void static_for(F&& f) { static_for_impl(f, std::make_integer_sequence<int, N>{}); }

constexpr int T_LEN = 4000, L_SEQ = 2000;
constexpr unsigned X_BYTES = 2 * T_LEN * 512, W_BYTES = 31 * 256 * 1024;
constexpr unsigned XF = 262144 + 128;
constexpr unsigned WF = 524288 + 128;
constexpr unsigned YT = 65536 + 128;
constexpr unsigned X_OFF = 0, W_OFF = (8u << 20) + 65536, Y_OFF = (24u << 20) + 131072;
constexpr unsigned F_OFF = (40u << 20) + 262144;
constexpr unsigned FK = 262144;
constexpr unsigned E_OFF = F_OFF + 2 * FK + 8192;
constexpr unsigned WS_USED = 42u << 20, OOB = 0x7ffffff0u, RS3 = 0x00020000;
constexpr int SC1 = 16;
constexpr int L_T2 = 36864, L_Z = 131072, L_G = 147456, LDS_BYTES = 155648;

DI unsigned pk(float a, float b) { bf16x2 r = {(__bf16)a, (__bf16)b}; return __builtin_bit_cast(unsigned, r); }
DI void cos_pair(float r0, float r1, float scale, unsigned& hi, unsigned& lo) {
    const float c0 = __builtin_amdgcn_cosf(r0) * scale, c1 = __builtin_amdgcn_cosf(r1) * scale;
    hi = pk(c0, c1);
    lo = pk(c0 - __builtin_bit_cast(float, hi << 16), c1 - __builtin_bit_cast(float, hi & 0xffff0000u));
}
DI u32x2 tr_read(const char* p) { return __builtin_bit_cast(u32x2, __builtin_amdgcn_ds_read_tr16_b64_v4i16((lds_s16x4_ptr)p)); }
DI unsigned sel(bool c, unsigned a, unsigned b) { unsigned r; asm("s_cmp_lg_u32 %1, 0\n\ts_cselect_b32 %0, %2, %3" : "=s"(r) : "s"((unsigned)c), "s"(a), "s"(b) : "scc"); return r; }
DI u32x4 make_rs(const void* p, unsigned bytes) { const u64 a = (u64)p; return u32x4{(unsigned)a, (unsigned)(a >> 32) & 0xffffu, bytes, RS3}; }

DI void m16_0(f32x4& d, u32x4 a, u32x4 b) { asm volatile("v_mfma_f32_16x16x32_bf16 %0, %1, %2, 0" : "=&v"(d) : "v"(a), "v"(b)); }
DI void m16(f32x4& d, u32x4 a, u32x4 b) { asm volatile("v_mfma_f32_16x16x32_bf16 %0, %1, %2, %0" : "+v"(d) : "v"(a), "v"(b)); }
DI void m32_0(f32x16& d, u32x4 a, u32x4 b) { asm volatile("v_mfma_f32_32x32x16_bf16 %0, %1, %2, 0" : "=&v"(d) : "v"(a), "v"(b)); }
DI void m32(f32x16& d, u32x4 a, u32x4 b) { asm volatile("v_mfma_f32_32x32x16_bf16 %0, %1, %2, %0" : "+v"(d) : "v"(a), "v"(b)); }
template <int R, bool FIRST> DI void m16_va(f32x4& d, u32x4 a) {
    if constexpr (FIRST) asm volatile("v_mfma_f32_16x16x32_bf16 %0, %1, a[%c2:%c3], 0" : "=&v"(d) : "v"(a), "i"(R), "i"(R + 3));
    else asm volatile("v_mfma_f32_16x16x32_bf16 %0, %1, a[%c2:%c3], %0" : "+v"(d) : "v"(a), "i"(R), "i"(R + 3));
}
template <int R, bool FIRST> DI void m32_av(f32x16& d, u32x4 b) {
    if constexpr (FIRST) asm volatile("v_mfma_f32_32x32x16_bf16 %0, a[%c2:%c3], %1, 0" : "=&v"(d) : "v"(b), "i"(R), "i"(R + 3));
    else asm volatile("v_mfma_f32_32x32x16_bf16 %0, a[%c2:%c3], %1, %0" : "+v"(d) : "v"(b), "i"(R), "i"(R + 3));
}
#define MFMA_DRAIN() do { GAP(); asm volatile("s_nop 7\n\ts_nop 7" ::: "memory"); GAP(); } while (0)
template <class A> DI void pin(A& a) { asm volatile("" : "+v"(a)); }
template <class A, class B> DI void pin(A& a, B& b) { asm volatile("" : "+v"(a), "+v"(b)); }

template <int N> DI void wait_vm() { asm volatile("s_waitcnt vmcnt(%0)" :: "i"(N) : "memory"); }
DI void wait_lds() { asm volatile("s_waitcnt lgkmcnt(0)" ::: "memory"); }
DI void barrier() { __builtin_amdgcn_s_barrier(); asm volatile("" ::: "memory"); }
DI void blds16(u32x4 rs, unsigned voff, unsigned soff, unsigned ldsaddr) {
    asm volatile("s_mov_b32 m0, %2\n\ts_nop 0\n\tbuffer_load_dwordx4 %0, %1, %3 offen lds" :: "v"(voff), "s"(rs), "s"(ldsaddr), "s"(soff) : "memory");
}
template <int OFF> DI void glds16_sc1(const char* sbase, unsigned voff, unsigned ldsaddr) {
    if constexpr (OFF == 0) asm volatile("s_mov_b32 m0, %2\n\ts_nop 0\n\tglobal_load_lds_dwordx4 %0, %1 sc1" :: "v"(voff), "s"(sbase), "s"(ldsaddr) : "memory");
    else asm volatile("global_load_lds_dwordx4 %0, %1 offset:%2 sc1" :: "v"(voff), "s"(sbase), "i"(OFF) : "memory");
}
template <int R, int OFF> DI void gload_a(const char* sbase, unsigned voff) {
    asm volatile("global_load_dwordx4 a[%c2:%c3], %0, %1 offset:%4 sc1" :: "v"(voff), "s"(sbase), "i"(R), "i"(R + 3), "i"(OFF) : "memory");
}
template <int R> DI void touch_a(const char* sbase, unsigned voff) {
    asm volatile("global_load_dword a%c2, %0, %1" :: "v"(voff), "s"(sbase), "i"(R) : "memory");
}
template <int R, int OFF> DI void tr_read_a(unsigned addr) {
    asm volatile("ds_read_b64_tr_b16 a[%c1:%c2], %0 offset:%3" :: "v"(addr), "i"(R), "i"(R + 1), "i"(OFF) : "memory");
}

DI void flag_set(rsrc_t rs, unsigned kind, unsigned voff, u32x2 tag) {
    __builtin_amdgcn_raw_buffer_store_b128(u32x4{tag.x, tag.y, tag.x, tag.y}, rs, voff, kind * FK, SC1);
}
template <int R> DI void poll_issue(const char* base, unsigned voff) {
    asm volatile("global_load_dwordx2 a[%c2:%c3], %0, %1 sc1" :: "v"(voff), "s"(base), "i"(R), "i"(R + 1) : "memory");
}
template <int R> DI u32x2 poll_value() {
    u32x2 v;
    asm volatile("v_accvgpr_read_b32 %0, a%c2\n\tv_accvgpr_read_b32 %1, a%c3\n\ts_nop 0" : "=v"(v.x), "=v"(v.y) : "i"(R), "i"(R + 1) : "memory");
    return v;
}
DI void poll_spin(const char* base, unsigned voff, u32x2 tag, u32x2 v) {
    const u64 t = __builtin_bit_cast(u64, tag);
    bool miss = __builtin_bit_cast(u64, v) != t;
    if (__builtin_expect(__builtin_amdgcn_ballot_w64(miss) != 0, 0)) {
        for (int spins = 0; spins < (1 << 16); ++spins) {
            __builtin_amdgcn_s_sleep(2);
            if (miss) {
                asm volatile("global_load_dwordx2 %0, %1, %2 sc1\n\ts_waitcnt vmcnt(0)" : "=&v"(v) : "v"(voff), "s"(base) : "memory");
                miss = __builtin_bit_cast(u64, v) != t;
            }
            if (__builtin_amdgcn_ballot_w64(miss) == 0) break;
        }
    }
}

template <bool X, int NCT, class H>
DI void dft_task(const char* img, int ta, const u32x4 (&fh)[2], const u32x4 (&fl)[2], rsrc_t rs, unsigned voff, H&& before_stores) {
    constexpr int NB = X ? 9 : 8;
    constexpr unsigned plane = X ? XF : WF;
    const unsigned voff1 = threadIdx.x >= 240 ? OOB : voff;
    u32x4 v[2][NB];
    f32x4 acc[2][8];
    auto reads = [&](int ct) INL {
        const char* t = img + (ta ^ (32 * ct));
#pragma unroll
        for (int b = 0; b < NB; ++b) {
            const u32x2 v0 = tr_read(t + b * 4096), v1 = tr_read(t + b * 4096 + 512);
            v[ct & 1][b] = u32x4{v0.x, v0.y, v1.x, v1.y};
        }
    };
    auto two = [&](int ct, int j) INL {
        f32x4 (&a)[8] = acc[ct & 1];
        const u32x4 (&u)[NB] = v[ct & 1];
        m16_0(a[j], fh[0], u[j]); m16_0(a[j + 1], fh[0], u[j + 1]); m16(a[j], fl[0], u[j]); m16(a[j + 1], fl[0], u[j + 1]);
        if constexpr (X) { m16(a[j], fh[1], u[j + 1]); m16(a[j + 1], fh[1], u[j + 2]); m16(a[j], fl[1], u[j + 1]); m16(a[j + 1], fl[1], u[j + 2]); }
    };
    auto out = [&](int ct) INL {
        f32x4 (&a)[8] = acc[ct & 1];
        asm volatile("" : "+v"(a[0]), "+v"(a[1]), "+v"(a[2]), "+v"(a[3]), "+v"(a[4]), "+v"(a[5]), "+v"(a[6]), "+v"(a[7]));
        const unsigned co = X ? (ct >> 1) * 4096 + (ct & 1) * 256 : ct * 32768;
#pragma unroll
        for (int i = 0; i < 4; ++i) {
            const u32x4 o = {pk(a[0][i], a[1][i]), pk(a[2][i], a[3][i]), pk(a[4][i], a[5][i]), pk(a[6][i], a[7][i])};
            __builtin_amdgcn_raw_buffer_store_b128(o, rs, (i >> 1) ? voff1 : voff, co + (i & 1) * 2048 + (i >> 1) * plane, SC1);
        }
    };
    reads(0);
    GAP();
#pragma unroll
    for (int ct = 0; ct < NCT; ++ct) {
        two(ct, 0);
        GAP();
        if (ct < NCT - 1) reads(ct + 1);
        GAP();
        two(ct, 2); two(ct, 4);
        GAP();
        if (NCT > 2 && ct == 1) before_stores();
        if (NCT > 2 && ct > 0) out(ct - 1);
        GAP();
        two(ct, 6);
        GAP();
    }
    MFMA_DRAIN();
    if (NCT == 2) { before_stores(); out(0); }
    out(NCT - 1);
    GAP();
}

struct XFrag { u32x2 a0, a1, b0, b1; };
constexpr int step_g(int st) { return st < 16 ? (st >> 1) & 3 : st < 32 ? (st >> 2) & 3 : (st - 32) >> 3; }
constexpr int step_ks(int st) { return st < 16 ? 2 * (st >> 3) + (st & 1) : st < 32 ? 4 + (st & 3) : 8 + 2 * (st & 3) + ((st >> 2) & 1); }

extern "C" __global__ __launch_bounds__(256, 1) void wavenet_fused(const char*, const char*, const char*, char*, char*) {
    u32x8 ka; u64 kb;
    asm volatile("s_load_dwordx8 %0, %2, 0x0\n\ts_load_dwordx2 %1, %2, 0x20" : "=&s"(ka), "=&s"(kb) : "s"(__builtin_amdgcn_kernarg_segment_ptr()) : "memory");
    extern __shared__ __attribute__((aligned(16))) char lds[];
    asm volatile("" ::: "a0", "a127", "a128", "a255");
    const int bid = blockIdx.x, tid = threadIdx.x, lane = tid & 63;
    const int wv = __builtin_amdgcn_readfirstlane(tid >> 6);
    {
        u64 pc; unsigned t0, t1;
        asm volatile("s_cmp_eq_u32 %4, 3\n\ts_cselect_b32 %1, %3, 32\n\ts_cmp_lt_u32 %1, 32\n\ts_cbranch_scc0 1f\n\t"
                     "s_getpc_b64 %0\n\t"
                     "global_load_dword a8, %5, %0\n\tv_add_u32 %2, 0x2000, %5\n\tglobal_load_dword a9, %2, %0\n\t"
                     "v_add_u32 %2, 0x4000, %5\n\tglobal_load_dword a10, %2, %0\n\t"
                     "v_add_u32 %2, 0x6000, %5\n\tv_min_u32 %2, 0x6400, %2\n\tglobal_load_dword a11, %2, %0\n"
                     "1:" : "=&s"(pc), "=&s"(t0), "=&v"(t1) : "s"(bid), "s"(wv), "v"(lane * 128) : "memory", "scc");
    }
    GAP();
    const unsigned ldsb = (unsigned)(size_t)(lds_ptr)lds;

    const bool xt = (bid >> 3) & 1;
    const int tk = (bid & 7) * 16 + (bid >> 4);
    const int srow = lane >> 3, schunk = (lane & 7) ^ (srow & 2);
    const int oc = tk >> 2, cq4 = tk & 3, sq = oc >> 3, o8 = oc & 7;
    const int oct = tk >> 3, dg = tk & 7, dg2 = dg ^ 1;
    const unsigned t_base = sel(xt, ((sq >> 1) * T_LEN + (sq & 1)) * 512 + cq4 * 128, 0);
    const unsigned t_r0 = sel(xt, (256 * o8 - 32 + 72 * wv) * 1024, oct * 8192 + dg * 128 + 2048 * wv);
    const unsigned t_sa = sel(xt, 8192, 2097152), t_sb = sel(xt, 32768, 1024), t_lim = sel(xt, L_SEQ * 1024, 0xffffffffu);
    const unsigned t_v0 = (srow << sel(xt, 10, 18)) + (schunk ^ ((wv & (bid >> 3) & 1) << 2)) * 16;
    unsigned t_so[9], t_v1 = t_v0 ^ 64, t_m0 = ldsb + wv * (8 + xt) * 1024;
#pragma unroll
    for (int i = 0; i < 9; ++i) {
        const unsigned r = t_r0 + (i & 3) * t_sa + (i >> 2) * t_sb;
        t_so[i] = r < t_lim ? t_base + r : X_BYTES;
        asm volatile("" : "+s"(t_so[i]));
    }
    asm volatile("" : "+v"(t_v1), "+s"(t_m0));
    GAP();
    asm volatile("s_waitcnt lgkmcnt(0)" : "+s"(ka), "+s"(kb) :: "memory");
    const char* w = (const char*)(((u64)ka[3] << 32) | ka[2]);
    const char* wo = (const char*)(((u64)ka[5] << 32) | ka[4]);
    char* ws = (char*)(((u64)ka[7] << 32) | ka[6]);
    char* out = (char*)kb;
    const char* fbase = ws + F_OFF + (bid & 7) * 32768;
    poll_issue<4>(ws, E_OFF);
    {
        const u32x4 rs = {sel(xt, ka[0], ka[2]), sel(xt, ka[1], ka[3]) & 0xffffu, sel(xt, X_BYTES, W_BYTES), RS3};
#define PIECE(v, s) "buffer_load_dwordx4 %" #v ", %2, %" #s " offen lds\n\ts_addk_i32 m0, 0x400\n\t"
        asm volatile("s_mov_b32 m0, %3\n\ts_nop 0\n\t"
                     PIECE(0, 5) "s_nop 0\n\t" PIECE(1, 6) "s_nop 0\n\t" PIECE(0, 7) "s_nop 0\n\t" PIECE(1, 8) "s_nop 0\n\t"
                     PIECE(0, 9) "s_nop 0\n\t" PIECE(1, 10) "s_nop 0\n\t" PIECE(0, 11) "s_nop 0\n\t" PIECE(1, 12)
                     "s_cmp_eq_u32 %4, 0\n\ts_cbranch_scc1 1f\n\tbuffer_load_dwordx4 %0, %2, %13 offen lds\n1:"
                     :: "v"(t_v0), "v"(t_v1), "s"(rs), "s"(t_m0), "s"((unsigned)xt), "s"(t_so[0]), "s"(t_so[1]), "s"(t_so[2]), "s"(t_so[3]),
                        "s"(t_so[4]), "s"(t_so[5]), "s"(t_so[6]), "s"(t_so[7]), "s"(t_so[8]) : "memory", "scc");
    }
    GAP();
    const int g4 = lane >> 4, q = (lane >> 2) & 3, p = lane & 3, col = lane & 15;
    const int hh = lane >> 5, r32 = lane & 31;
    const rsrc_t rs_ws = __builtin_amdgcn_make_buffer_rsrc((void*)ws, 0, WS_USED, RS3);
    u32x4 fh[2], fl[2];
    {
        const int rho = 16 * wv + col;
        const float st = (float)(rho | 1) * (1.0f / 124.0f), r0 = __builtin_fmaf(st, (float)(8 * g4), (rho & 1) ? 0.25f : 0.0f);
#pragma unroll
        for (int kb2 = 0; kb2 < 2; ++kb2)
#pragma unroll
            for (int j = 0; j < 4; ++j) {
                const int n = 32 * kb2 + 2 * j;
                unsigned a, b; cos_pair(__builtin_fmaf(st, (float)n, r0), __builtin_fmaf(st, (float)(n + 1), r0), 1.0f, a, b); fh[kb2][j] = a; fl[kb2][j] = b;
            }
        asm volatile("" : "+v"(fh[0]), "+v"(fh[1]), "+v"(fl[0]), "+v"(fl[1]));
    }
    GAP();
    if (bid < 8 && wv == 0) {
        wait_vm<8>();
        u32x2 t = poll_value<4>();
        t.x += 1;
        flag_set(rs_ws, 2, F_OFF + srow * 32768 + bid * 128 + (lane & 7) * 16, t);
    }
    {
        const u32x4 rs = make_rs(w, W_BYTES);
        const unsigned v0 = srow * 262144 + schunk * 16, v1 = v0 ^ 64, base = 131072 + oct * 8192 + dg2 * 128 + 2048 * wv;
#pragma unroll
        for (int i = 0; i < 8; ++i) blds16(rs, (i & 1) ? v1 : v0, base + (i >> 2) * 1024 + (i & 3) * 2097152, ldsb + L_T2 + (wv * 8 + i) * 1024);
    }
    {
        const float st = (float)(32 + r32) * (1.0f / 124.0f), r0 = st * (float)(16 * wv + 8 * hh + 1);
        u32x4 a, b;
#pragma unroll
        for (int j = 0; j < 4; ++j) {
            const float r = __builtin_fmaf(st, (float)(2 * j), r0);
            unsigned u, v; cos_pair(r, r + 0.25f, (j == 3 && tid >= 224) ? 0.0f : 1.4426950408889634f / 31.0f, u, v); a[j] = u; b[j] = v;
        }
        *(u32x4*)(lds + L_G + wv * 2048 + lane * 16) = a;
        *(u32x4*)(lds + L_G + wv * 2048 + 1024 + lane * 16) = b;
        wait_lds();
    }
    const int ta = (8 * g4 + q) * 128 + (((q >> 1) | ((g4 & 1) << 1)) * 32) + 8 * p;
    const int f0 = 8 * wv + 2 * g4;
    GAP();
    wait_vm<8>(); barrier();
    const unsigned po0 = 2 * FK + (lane & 7) * 128;
    poll_issue<2>(fbase, po0);
    touch_a<12>(wo, ((bid >> 3) * 32 + 8 * wv + (lane & 7)) * 128);
    u32x2 tag = poll_value<4>();
    tag.x = __builtin_amdgcn_readfirstlane(tag.x) + 1;
    tag.y = __builtin_amdgcn_readfirstlane(tag.y);
    const u32x2 notag = {~tag.x, tag.y};
    const unsigned myflag = lane < 16 ? F_OFF + (2 * wv + srow) * 32768 + bid * 128 + (lane & 7) * 16 : OOB;
    auto publish = [&](int kind) INL { barrier(); flag_set(rs_ws, kind, myflag, tag); };
    auto all_started = [&]() INL { wait_vm<1>(); poll_spin(fbase, po0, tag, poll_value<2>()); };
    GAP();
    const unsigned wvo = W_OFF + f0 * WF + (oct >> 2) * 4096 + (oct & 3) * 256 + col * 16;
    if (xt) {
        const int og = o8 & 1;
        u32x4 xh[2] = {fh[0], fh[1]}, xl[2] = {fl[0], fl[1]};
        xh[0].x = g4 ? xh[0].x : 0; xl[0].x = g4 ? xl[0].x : 0;
        asm volatile("s_nop 1" : "+v"(xh[0]), "+v"(xl[0]));
        dft_task<true, 4>(lds, ta, xh, xl, rs_ws, X_OFF + f0 * XF + (oc >> 2) * 32768 + cq4 * 8192 + (o8 & 3) * 512 + ((col ^ (4 * og)) * 16), all_started);
    } else dft_task<false, 4>(lds, ta, fh, fl, rs_ws, wvo + (dg & 3) * 131072 + (dg >> 2) * 1024, all_started);
    GAP();
    barrier();
    GAP();
    dft_task<false, 2>(lds + L_T2, ta ^ (xt ? 64 : 0), fh, fl, rs_ws, wvo + 16384 + (dg2 & 3) * 131072 + (dg2 >> 2) * 1024 + (xt ? 65536 : 0),
                       [&]() INL { wait_vm<0>(); publish(0); GAP(); });
    constexpr int NW1 = 12;
    const unsigned l16 = lane * 16;
    const u32x4 rs_o = make_rs(wo, 131072);
    const unsigned ovo = srow * 512 + wv * 128 + ((lane & 7) ^ (srow & 4)) * 16;
    const unsigned ost1 = ldsb + wv * 32768, ost0 = ost1 + 16384, ostz = ldsb + L_Z + wv * 4096 - NW1 * 1024;
    auto o_issue = [&](auto I) INL { constexpr int i = I.value; blds16(rs_o, ovo, i * 4096, (i < NW1 ? ost0 : i < 16 ? ostz : ost1) + (i & 15) * 1024); };
    const unsigned ora = ((q & 1) + 4 * (q >> 1) + 2 * hh) * 128 + ((32 * (g4 & 1) + 8 * p) ^ (64 * (q >> 1)));
    auto o_read = [&](auto FROM, auto TO, unsigned base) INL {
        const unsigned a0 = base + ora, a1 = a0 ^ 64;
        static_for<TO.value - FROM.value>([&](auto I) INL {
            constexpr int i = FROM.value + I.value;
            tr_read_a<128 + 8 * (i >> 1) + 2 * (i & 1), (i & 15) * 1024>(a0);
            tr_read_a<132 + 8 * (i >> 1) + 2 * (i & 1), (i & 15) * 1024>(a1);
        });
    };
    static_for<NW1>(o_issue);
    GAP();

    const int f = (bid & 7) * 4 + (bid >> 6), th = (bid >> 5) & 1, cg = ((bid >> 3) & 3) * 4 + wv;
    unsigned po1, po2;
    {
        const int tka = 16 * (4 * th + wv) + (lane & 15), tkw = 8 * (lane & 15) + (cg >> 2) + ((lane & 16) >> 2), tku = tkw ^ 1;
        po1 = (lane & 32) ? ((tka >> 4) | 8 | ((tka & 15) << 4)) * 128 : ((tkw >> 4) | ((tkw & 15) << 4)) * 128;
        po2 = ((tku >> 4) | ((wv & 2) << 2) | ((tku & 15) << 4)) * 128 + FK;
    }
    const int s3 = bid >> 6, k3 = bid & 63, rf = r32 < 31 ? r32 : 30;
    const unsigned pa = ((rf >> 2) | ((4 * (bid >> 7) + wv) << 3) | ((rf & 3) << 6)) * 128 + (3 + ((bid >> 5) & 3)) * FK;
    const bool early = !(bid & 64);
    poll_issue<0>(fbase, po1);
    wait_vm<NW1 + 1>(); publish(1);
    wait_vm<1>();
    GAP();
    if (f == 31) {
        static_for<32 - NW1>([&](auto I) INL { o_issue(IC(NW1 + I.value)); });
        wait_vm<0>();
        o_read(IC(0), IC(NW1), ost0); o_read(IC(NW1), IC(16), ostz); o_read(IC(16), IC(32), ost1);
        wait_lds();
        __builtin_amdgcn_s_sleep(127);
        poll_spin(fbase, 4 * FK + (bid - 64) * 128, tag, notag);
        __builtin_amdgcn_s_sleep(42);
    } else {
        poll_spin(fbase, po1, tag, poll_value<0>());
        GAP();
        const char* wsrc = ws + W_OFF + f * WF + cg * 32768 + 4096;
        const char* xsrc = ws + X_OFF + f * XF + (4 * th + wv) * 32768;
        const char* usrc = ws + X_OFF + f * XF + 4 * th * 32768 + 16384 + wv * 4096;
        auto w_issue = [&](auto I) INL { constexpr int i = I.value; gload_a<4 * i, (i & 7) * 1024 - 4096>(wsrc + (i >> 3) * 8192, l16); };
        auto x_issue = [&](auto I) INL { constexpr int i = I.value; glds16_sc1<(i & 3) * 1024>(xsrc + (i >> 2) * 4096, l16, ost1 + (i >> 2) * 4096); };
        auto u_issue = [&](auto I) INL { constexpr int i = I.value; glds16_sc1<(i & 3) * 1024>(usrc + (i >> 2) * 32768, l16, ldsb + 16384 + wv * 4096 + (i >> 2) * 32768); };
        static_for<4>([&](auto I) INL { x_issue(I); });
        static_for<4>([&](auto I) INL { w_issue(I); });
        o_read(IC(0), IC(NW1), ost0);
        wait_lds();
        static_for<4>([&](auto I) INL { w_issue(IC(4 + I.value)); });
        static_for<4>([&](auto I) INL { x_issue(IC(4 + I.value)); });
        GAP();
        const int og = p >> 1;
        const int tb = og * 512 + (((8 * g4 + q) ^ (4 * og)) * 16) + 8 * (p & 1);
        int tb2 = tb + 65536, tb3 = (tb ^ 64) + 65536;
        pin(tb2, tb3);
        const char* tx[4] = {lds + tb, lds + (tb ^ 64), lds + tb2, lds + tb3};
        const unsigned yoff = Y_OFF + (128 * th + 4 * g4) * YT + f * 2048 + cg * 128 + col * 8;
        auto rd = [&](XFrag& xx, int st, int part) INL {
            const int g = step_g(st), o = (g & 1) * 32768 + (step_ks(st) >> 1) * 4096 + (step_ks(st) & 1) * 1024;
            if (part == 0) xx.a0 = tr_read(tx[g & 2] + o);
            if (part == 1) xx.a1 = tr_read(tx[(g & 2) + 1] + o);
            if (part == 2) xx.b0 = tr_read(tx[g & 2] + o + 2048);
            if (part == 3) xx.b1 = tr_read(tx[(g & 2) + 1] + o + 2048);
        };
        f32x4 p1[4][2][2], p2[4][2][2], p3[4][2][2];
        u32x2 yv;
        auto y_pack = [&](int g, int e, int ct) INL {
            unsigned v = pk(p1[g][e >> 2][ct][e & 3] - p2[g][e >> 2][ct][e & 3], p3[g][e >> 2][ct][e & 3]);
            pin(v);
            yv[ct] = v;
        };
        auto y_store = [&](int g, int e) INL { __builtin_amdgcn_raw_buffer_store_b64(yv, rs_ws, yoff, (32 * g + 16 * (e >> 2) + (e & 3)) * YT, SC1); };
        auto y_pin = [&](int g, int rt) INL { pin(p1[g][rt][0], p1[g][rt][1]); pin(p2[g][rt][0], p2[g][rt][1]); pin(p3[g][rt][0], p3[g][rt][1]); };
        XFrag xf[3];
        wait_vm<8>(); barrier();
        GAP();
#pragma unroll
        for (int part = 0; part < 4; ++part) rd(xf[0], 0, part);
#pragma unroll
        for (int part = 0; part < 4; ++part) rd(xf[1], 1, part);
        GAP();
        static_for<64>([&](auto ST) INL {
            constexpr int st = ST.value, g = step_g(st), ks = step_ks(st);
            if constexpr (st == 6) { wait_vm<12>(); barrier(); }
            if constexpr (st == 8) poll_issue<126>(fbase, po2);
            if constexpr (st == 12) { wait_vm<8>(); poll_spin(fbase, po2, tag, poll_value<126>()); }
            if constexpr (st == 14) { barrier(); GAP(); }
            if constexpr (st == 30) { wait_vm<36 - NW1>(); wait_lds(); barrier(); GAP(); }
            if constexpr (st == 34) { wait_vm<32 - NW1>(); GAP(); }
            if constexpr (st == 38) { wait_vm<29 - NW1>(); barrier(); }
            if constexpr (st == 50 || st == 58) { wait_vm<5>(); publish(st == 50 ? 3 : 4); GAP(); }
            auto vm = [&](auto H) INL {
                constexpr int h = H.value;
                if constexpr (st < 4) w_issue(IC(8 + 2 * st + h));
                else if constexpr (st < 8) x_issue(IC(2 * st + h));
                else if constexpr (st < 10) u_issue(IC(2 * (st - 8) + h));
                else if constexpr (st < 12) flag_set(rs_ws, 3 + 2 * (st - 10) + h, myflag, notag);
                else if constexpr (st < 20) w_issue(IC(16 + 2 * (st - 12) + h));
                else if constexpr (st < 26) u_issue(IC(4 + 2 * (st - 20) + h));
                else if constexpr (NW1 + 2 * (st - 26) + h < 16) o_issue(IC(NW1 + 2 * (st - 26) + h));
                else if constexpr (st >= 32 && st < 40) o_issue(IC(16 + 2 * (st - 32) + h));
            };
            constexpr bool ep = st >= 36, F = ks < 2;
            constexpr int e0 = st - 36, rt = ks & 1, R = 16 * (ks >> 1);
            const XFrag& xx = xf[st % 3];
            XFrag& nx = xf[(st + 2) % 3];
            const u32x4 xr = {xx.a0.x, xx.a0.y, xx.a1.x, xx.a1.y}, xi = {xx.b0.x, xx.b0.y, xx.b1.x, xx.b1.y};
            m16_va<R, F>(p1[g][rt][0], xr);
            GAP();
            if (st < 62) rd(nx, st + 2, 0);
            GAP();
            m16_va<R, F>(p3[g][rt][0], xi);
            GAP();
            if constexpr (ep && e0 > 0) y_store((e0 - 1) >> 3, (e0 - 1) & 7);
            GAP();
            m16_va<R + 4, F>(p3[g][rt][1], xi);
            GAP();
            if (st < 62) rd(nx, st + 2, 1);
            GAP();
            m16_va<R + 4, F>(p1[g][rt][1], xr);
            GAP();
            vm(IC(0));
            if constexpr (ep && (e0 & 3) == 0) y_pin(e0 >> 3, (e0 >> 2) & 1);
            if constexpr (ep) y_pack(e0 >> 3, e0 & 7, 0);
            GAP();
            m16_va<R + 8, false>(p3[g][rt][0], xr);
            GAP();
            if (st < 62) rd(nx, st + 2, 2);
            GAP();
            m16_va<R + 8, F>(p2[g][rt][0], xi);
            GAP();
            if constexpr (ep) y_pack(e0 >> 3, e0 & 7, 1);
            GAP();
            m16_va<R + 12, F>(p2[g][rt][1], xi);
            GAP();
            if (st < 62) rd(nx, st + 2, 3);
            GAP();
            m16_va<R + 12, false>(p3[g][rt][1], xr);
            GAP();
            vm(IC(1));
            GAP();
        });
        y_store(3, 3);
        MFMA_DRAIN();
        y_pin(3, 1);
#pragma unroll
        for (int e = 4; e < 8; ++e) { y_pack(3, e, 0); y_pack(3, e, 1); y_store(3, e); }
        GAP();
        wait_vm<8>(); publish(5);
        o_read(IC(NW1), IC(16), ostz); o_read(IC(16), IC(32), ost1);
        wait_lds();
        if (early) { poll_issue<0>(fbase, pa); wait_vm<2>(); } else wait_vm<1>();
        publish(6);
    }
    GAP();

    if (k3 == 63) {
        if (bid == 255) {
            poll_spin(fbase, (wv * 64 + lane) * 128, tag, notag);
            barrier();
            flag_set(rs_ws, 0, tid < 8 ? E_OFF + tid * 16 : OOB, tag);
        }
        return;
    }
    unsigned voff = Y_OFF + bid * YT + hh * 8192 + 512 * wv + r32 * 4;
    unsigned voff1 = hh ? OOB - 1024 : voff;
    unsigned ooff[4];
#pragma unroll
    for (int i = 0; i < 4; ++i) {
        const int m = 32 * k3 + 8 * i + srow;
        ooff[i] = m < L_SEQ ? ((s3 >> 1) * T_LEN + (s3 & 1)) * 512 + 128 * wv + (lane & 7) * 16 + m * 1024 : OOB;
    }
    u32x4 y[4][4], gh[4], gl[4];
    auto y_load = [&](int n) INL {
        y[n >> 4][(n >> 2) & 3][n & 3] = __builtin_amdgcn_raw_buffer_load_b32(rs_ws, ((n & 15) == 15 ? voff1 : voff) + (n >> 4) * 128, ((n >> 2) & 3) * 16384 + (n & 3) * 2048, SC1);
    };
    auto y_more = [&](int slot) INL { if (slot < 16) { y_load(32 + 2 * slot); y_load(33 + 2 * slot); } };
#pragma unroll
    for (int ks = 0; ks < 4; ++ks) {
        gh[ks] = *(const u32x4*)(lds + L_G + ks * 2048 + lane * 16);
        gl[ks] = *(const u32x4*)(lds + L_G + ks * 2048 + 1024 + lane * 16);
        pin(gh[ks], gl[ks]);
    }
    pin(voff, voff1); pin(ooff[0], ooff[1]); pin(ooff[2], ooff[3]);
    GAP();
    if (early) wait_vm<1>();
    else {
        if (bid & 32) __builtin_amdgcn_s_sleep(26); else __builtin_amdgcn_s_sleep(6);
        poll_issue<0>(fbase, pa); wait_vm<0>();
    }
    poll_spin(fbase, pa, tag, poll_value<0>());
    GAP();
#pragma unroll
    for (int n = 0; n < 32; ++n) { y_load(n); if ((n & 15) == 15) GAP(); }
    char* zb = lds + L_Z;
    f32x16 h[2], o0, o1;
    float z[8], gc[2], gb[2];
    u32x4 zf[12];
    auto inv = [&](int sg, int i) INL {
        if (i == 0) m32_0(h[sg & 1], y[sg][0], gh[0]); else m32(h[sg & 1], y[sg][i >> 1], (i & 1) ? gl[i >> 1] : gh[i >> 1]);
    };
    auto gate = [&](int sg, int i) INL {
        const int e = i & ~1;
        const f32x16& hs = h[sg & 1];
        if (!(i & 1)) {
#pragma unroll
            for (int k = 0; k < 2; ++k) {
                gc[k] = __builtin_amdgcn_exp2f(__builtin_amdgcn_fmed3f(hs[2 * (e + k)], -30.0f, 30.0f));
                gb[k] = __builtin_amdgcn_exp2f(-hs[2 * (e + k) + 1]);
            }
            pin(gc[0], gc[1]); pin(gb[0], gb[1]);
        } else {
#pragma unroll
            for (int k = 0; k < 2; ++k) {
                const float t = __builtin_fmaf(gc[k], gc[k], 1.0f), u = __builtin_fmaf(gc[k], gc[k], -1.0f);
                z[e + k] = u * __builtin_amdgcn_rcpf(__builtin_fmaf(t, gb[k], t));
            }
            pin(z[e], z[e + 1]);
        }
    };
    auto z_put = [&](int sg) INL {
        *(u32x4*)(zb + (4 * wv + sg) * 1024 + lane * 16) = u32x4{pk(z[0], z[1]), pk(z[2], z[3]), pk(z[4], z[5]), pk(z[6], z[7])};
        GAP();
    };
    auto z_get = [&](int sg, int slot) INL {
#pragma unroll
        for (int i = 0; i < 4; ++i) zf[slot + i] = *(const u32x4*)(zb + (4 * i + sg) * 1024 + lane * 16);
        GAP();
    };
    auto proj = [&](auto N) INL {
        constexpr int sg = N.value >> 3, i = N.value & 7, kz = 4 * (i >> 1) + sg, slot = 4 * (sg % 3) + (i >> 1);
        if constexpr (i & 1) m32_av<132 + 8 * kz, kz == 0>(o1, zf[slot]); else m32_av<128 + 8 * kz, kz == 0>(o0, zf[slot]);
    };
#pragma unroll
    for (int i = 0; i < 8; ++i) { inv(0, i); GAP(); y_more(i); GAP(); }
#pragma unroll
    for (int sg = 1; sg < 4; ++sg) {
        inv(sg, 0);
        if (sg == 1) asm volatile("s_nop 7\n\ts_nop 2" : "+v"(h[0])); else pin(h[(sg - 1) & 1]);
        GAP();
#pragma unroll
        for (int i = 0; i < 8; ++i) { gate(sg - 1, i); y_more(8 * sg + i); GAP(); if (i < 7) inv(sg, i + 1); GAP(); }
        z_put(sg - 1);
    }
    wait_lds(); barrier();
    z_get(0, 0); z_get(1, 4); z_get(2, 8);
    pin(h[1]);
    GAP();
    static_for<8>([&](auto I) INL {
        gate(3, I.value);
        GAP();
        proj(IC(3 * I.value)); proj(IC(3 * I.value + 1));
        if constexpr (I.value < 7) proj(IC(3 * I.value + 2));
        GAP();
    });
    z_put(3);
    proj(IC(23));
    wait_lds(); barrier();
    z_get(3, 0);
    static_for<8>([&](auto I) INL { proj(IC(24 + I.value)); });
    MFMA_DRAIN();
    pin(o0, o1);
    GAP();
    {
        char* ob = lds + wv * 4096;
#pragma unroll
        for (int et = 0; et < 2; ++et) {
            const f32x16& o = et ? o1 : o0;
#pragma unroll
            for (int mm = 0; mm < 4; ++mm)
                *(u32x2*)(ob + r32 * 128 + (((4 * et + mm) ^ (r32 & 7)) * 16) + 8 * hh) = u32x2{pk(o[4 * mm], o[4 * mm + 1]), pk(o[4 * mm + 2], o[4 * mm + 3])};
        }
        const rsrc_t rs_out = __builtin_amdgcn_make_buffer_rsrc((void*)out, 0, X_BYTES, RS3);
#pragma unroll
        for (int i = 0; i < 4; ++i)
            __builtin_amdgcn_raw_buffer_store_b128(*(const u32x4*)(ob + (8 * i + srow) * 128 + (((lane & 7) ^ srow) * 16)), rs_out, ooff[i], 0, SC1);
    }
}

extern "C" void kernel_launch(void* const* d_in, const int* in_sizes, int n_in,
                              void* d_out, int out_size, void* d_ws, size_t ws_size,
                              hipStream_t stream) {
    (void)hipFuncSetAttribute((const void*)wavenet_fused, hipFuncAttributeMaxDynamicSharedMemorySize, LDS_BYTES);
    wavenet_fused<<<256, 256, LDS_BYTES, stream>>>((const char*)d_in[0], (const char*)d_in[1], (const char*)d_in[2], (char*)d_ws, (char*)d_out);
}
```
